```python
import torch

def _softmax(x, axis=-1):
    return torch.softmax(x, dim=axis)
def _gelu(x):
    return torch.nn.functional.gelu(x, approximate="tanh")
def _silu(x):
    return torch.nn.functional.silu(x)
def _sigmoid(x):
    return torch.sigmoid(x)
def _layernorm(x, axis=-1, eps=1e-5):
    m = x.mean(dim=axis, keepdim=True)
    v = x.var(dim=axis, keepdim=True, unbiased=False)
    return (x - m) / torch.sqrt(v + eps)
def _scatter_add(tgt, idx, src, axis=0):
    return tgt.clone().index_add_(axis, idx, src)
def _segment_sum(src, seg, n):
    out = torch.zeros((n,) + src.shape[1:], dtype=src.dtype)
    return out.index_add_(0, seg, src)
def _pad(x, pw):
    flat = tuple(v for pair in reversed(pw) for v in pair)
    return torch.nn.functional.pad(x, flat)

kernel_name = 'recurrent_scan__sliding_block_cached__glu_gated_reglu__decode'

def setup_inputs():

    _w = (lambda a: (lambda t: t.to(torch.bfloat16) if t.is_floating_point() else t)(torch.as_tensor(a)))
    def _A_setup():
        B, Sq, D = 4, 1, 448
        Hq, Hkv, Sk, Dh = 16, 8, 8192, 256
        assert Hq % Hkv == 0, (Hq, Hkv)
        return {
            "x": _w((lambda *s: torch.randn(*s, dtype=torch.bfloat16) * 0.05)(B, Sq, D)),
            "k_cache": _w((lambda *s: torch.randn(*s, dtype=torch.bfloat16) * 3.2)(B, Hkv, Sk, Dh)),
            "v_cache": _w((lambda *s: torch.randn(*s, dtype=torch.bfloat16) * 3.2)(B, Hkv, Sk, Dh)),
            "wq": _w((lambda *s: torch.randn(*s, dtype=torch.bfloat16) * 3.2)(D, Hq * Dh) * (D ** -0.5)),
            "wo": _w((lambda *s: torch.randn(*s, dtype=torch.bfloat16) * 3.2)(Hq * Dh, D) * ((Hq * Dh) ** -0.5)),
        }
    _A_in = _A_setup()

    def _B_setup():
        B, S, D, F = 4, 1, 448, 1792
        return {
            "x": _w((lambda *s: torch.randn(*s, dtype=torch.bfloat16) * 0.05)(B, S, D)),
            "W_gate": _w((lambda *s: torch.randn(*s, dtype=torch.bfloat16) * 0.4)(D, F) * (2.0 / D) ** 0.5),
            "W_up": _w((lambda *s: torch.randn(*s, dtype=torch.bfloat16) * 0.4)(D, F) * (2.0 / D) ** 0.5),
            "W_down": _w((lambda *s: torch.randn(*s, dtype=torch.bfloat16) * 0.4)(F, D) * (1.0 / F) ** 0.5),
        }
    _B_in = _B_setup()

    return {
        "A_x": _A_in["x"],
        "A_k_cache": _A_in["k_cache"],
        "A_v_cache": _A_in["v_cache"],
        "A_wq": _A_in["wq"],
        "A_wo": _A_in["wo"],
        "B_x": _B_in["x"],
        "B_W_gate": _B_in["W_gate"],
        "B_W_up": _B_in["W_up"],
        "B_W_down": _B_in["W_down"],
    }

def reference(A_x, A_k_cache, A_v_cache, A_wq, A_wo, B_x, B_W_gate, B_W_up, B_W_down):
    def _A_ref(x, k_cache, v_cache, wq, wo):
        B, Sq, D = x.shape
        Hq, Dh = 16, 256
        Hkv, Sk = k_cache.shape[1], k_cache.shape[2]
        W = min(4096, max(256, Sk // 4))
        rep = Hq // Hkv
        q = (lambda x, *d: x.permute(*d))((x @ wq).reshape(B, Sq, Hq, Dh), 0, 2, 1, 3)
        k = (lambda x, n, axis: torch.repeat_interleave(x, n, dim=axis))(k_cache[:, :, Sk - W:, :], rep, axis=1)
        v = (lambda x, n, axis: torch.repeat_interleave(x, n, dim=axis))(v_cache[:, :, Sk - W:, :], rep, axis=1)
        scores = torch.einsum("bhqd,bhkd->bhqk", q, k) * (Dh ** -0.5)
        p = _softmax(scores, axis=-1)
        o = torch.einsum("bhqk,bhkd->bhqd", p, v)
        return (lambda x, *d: x.permute(*d))(o, 0, 2, 1, 3).reshape(B, Sq, Hq * Dh) @ wo

    def _B_ref(x, W_gate, W_up, W_down):
        gate = torch.einsum("bsd,df->bsf", x, W_gate)
        up = torch.einsum("bsd,df->bsf", x, W_up)
        gate = gate * (lambda x, dt: x.to(dt))(gate > 0, gate.dtype)
        h = gate * up
        return torch.einsum("bsf,fd->bsd", h, W_down)

    x = A_x
    xn = _layernorm(x, axis=-1)
    s = _A_ref(xn, A_k_cache, A_v_cache, A_wq, A_wo)
    g = _silu(_B_ref(xn, B_W_gate, B_W_up, B_W_down))
    return x + s * g
```

```cpp
#include <hip/hip_runtime.h>
#include <hip/hip_bf16.h>
#include <stdint.h>

typedef unsigned u32x4 __attribute__((ext_vector_type(4)));
typedef float f32x4 __attribute__((ext_vector_type(4)));
typedef __bf16 bf16x8 __attribute__((ext_vector_type(8)));
typedef unsigned short u16;

constexpr int B = 4, D = 448, HQ = 16, HKV = 8, SK = 8192, DH = 256, W = 2048, F = 1792;
constexpr int NSPLIT = 8, KEYS = W / NSPLIT;
#define KV_NT 1

__device__ __forceinline__ float bf2f(u16 x) { return __uint_as_float((unsigned)x << 16); }
__device__ __forceinline__ float bflo(unsigned x) { return __uint_as_float(x << 16); }
__device__ __forceinline__ float bfhi(unsigned x) { return __uint_as_float(x & 0xffff0000u); }
__device__ __forceinline__ u16 f2bf(float x) { return __builtin_bit_cast(u16, (__bf16)x); }
__device__ __forceinline__ float rbf(float x) { return (float)(__bf16)x; }
__device__ __forceinline__ u32x4 ldkv(const u32x4* p) { return KV_NT ? __builtin_nontemporal_load(p) : *p; }
__device__ __forceinline__ f32x4 mfma16(u32x4 a, u32x4 b, f32x4 c) {
  return __builtin_amdgcn_mfma_f32_16x16x32_bf16(__builtin_bit_cast(bf16x8, a), __builtin_bit_cast(bf16x8, b), c, 0, 0, 0);
}
__device__ __forceinline__ float wave_sum(float x) {
#pragma unroll
  for (int o = 32; o > 0; o >>= 1) x += __shfl_xor(x, o);
  return x;
}

__device__ __forceinline__ void layernorm_lds(const u16* x, float* xn, int wave, int lane) {
  if (wave < B) {
    float v[7], s = 0.f;
#pragma unroll
    for (int i = 0; i < 7; ++i) { v[i] = bf2f(x[wave * D + lane + 64 * i]); s += v[i]; }
    const float mean = wave_sum(s) * (1.f / D);
    float q = 0.f;
#pragma unroll
    for (int i = 0; i < 7; ++i) q += (v[i] - mean) * (v[i] - mean);
    const float var = wave_sum(q) * (1.f / D);
    const float den = rbf(sqrtf(rbf(rbf(var) + 1e-5f)));
    const float mb = rbf(mean);
#pragma unroll
    for (int i = 0; i < 7; ++i) xn[wave * D + lane + 64 * i] = rbf(rbf(v[i] - mb) / den);
  }
}

__global__ void __launch_bounds__(512) k_proj(const u16* x, const u16* wq, const u16* wg, const u16* wu, u16* q, float* h) {
  __shared__ float xn[B * D];
  __shared__ float red[2][64][B][64 + 1];
  const int tid = threadIdx.x, wave = tid >> 6, lane = tid & 63;
  layernorm_lds(x, xn, wave, lane);
  __syncthreads();
  const bool isq = blockIdx.x < 64;
  const int c0 = isq ? blockIdx.x * 64 : (blockIdx.x - 64) * 64;
  const int ld = isq ? HQ * DH : F;
  const int rg = tid >> 3, cg = tid & 7;
  for (int mat = 0; mat < (isq ? 1 : 2); ++mat) {
    const u16* w = isq ? wq : (mat ? wu : wg);
    float acc[B][8];
#pragma unroll
    for (int b = 0; b < B; ++b)
#pragma unroll
      for (int j = 0; j < 8; ++j) acc[b][j] = 0.f;
#pragma unroll
    for (int i = 0; i < 7; ++i) {
      const int r = rg + 64 * i;
      const u32x4 wv = *(const u32x4*)(w + (size_t)r * ld + c0 + cg * 8);
#pragma unroll
      for (int b = 0; b < B; ++b) {
        const float xv = xn[b * D + r];
#pragma unroll
        for (int j = 0; j < 4; ++j) { acc[b][2 * j] += xv * bflo(wv[j]); acc[b][2 * j + 1] += xv * bfhi(wv[j]); }
      }
    }
#pragma unroll
    for (int b = 0; b < B; ++b)
#pragma unroll
      for (int j = 0; j < 8; ++j) red[mat][rg][b][cg * 8 + j] = acc[b][j];
  }
  __syncthreads();
  if (tid < 256) {
    const int b = tid >> 6, c = tid & 63;
    float s0 = 0.f, s1 = 0.f;
    for (int r = 0; r < 64; ++r) { s0 += red[0][r][b][c]; if (!isq) s1 += red[1][r][b][c]; }
    if (isq) q[b * HQ * DH + c0 + c] = f2bf(s0);
    else { const float g = rbf(s0), u = rbf(s1); h[b * F + c0 + c] = rbf((g > 0.f ? g : 0.f) * u); }
  }
}

__global__ void __launch_bounds__(512) k_attn(const u16* q, const u16* kc, const u16* vc, float* po, float* pml) {
  __shared__ float so[8][512];
  __shared__ float sml[8][4];
  const int wg = blockIdx.x, tid = threadIdx.x, wave = tid >> 6, lane = tid & 63;
  const int kvh = wg & 7, b = wg >> 6, sp = (wg >> 3) & 7;
  const int n = lane & 15, g = lane >> 4;
  const size_t row0 = (size_t)(b * HKV + kvh) * SK + (SK - W) + sp * KEYS + wave * 32;
  const u32x4* kp = (const u32x4*)kc + (row0 + n) * 32 + g;
  const u32x4* vp = (const u32x4*)vc + (row0 + 4 * g) * 32 + n;
  u32x4 qf[8], kf[2][8], vf[2][4][2];
  const u32x4* qp = (const u32x4*)(q + b * HQ * DH + (kvh * 2 + (n >> 3)) * DH) + g;
#pragma unroll
  for (int i = 0; i < 8; ++i) qf[i] = qp[4 * i];
#pragma unroll
  for (int t = 0; t < 2; ++t)
#pragma unroll
    for (int i = 0; i < 8; ++i) kf[t][i] = ldkv(kp + t * 16 * 32 + 4 * i);
#pragma unroll
  for (int t = 0; t < 2; ++t)
#pragma unroll
    for (int r = 0; r < 4; ++r)
#pragma unroll
      for (int hf = 0; hf < 2; ++hf) vf[t][r][hf] = ldkv(vp + (t * 16 + r) * 32 + 16 * hf);
  __builtin_amdgcn_sched_barrier(0);
  f32x4 s[2];
#pragma unroll
  for (int t = 0; t < 2; ++t) {
    s[t] = f32x4{0.f, 0.f, 0.f, 0.f};
#pragma unroll
    for (int i = 0; i < 8; ++i) s[t] = mfma16(kf[t][i], qf[i], s[t]);
  }
  constexpr float C2 = 0.0625f * 1.4426950408889634f;
  float m = -INFINITY;
#pragma unroll
  for (int t = 0; t < 2; ++t)
#pragma unroll
    for (int r = 0; r < 4; ++r) { s[t][r] *= C2; m = fmaxf(m, s[t][r]); }
  m = fmaxf(m, __shfl_xor(m, 16));
  m = fmaxf(m, __shfl_xor(m, 32));
  unsigned msk[4];
#pragma unroll
  for (int i = 0; i < 4; ++i) msk[i] = ((n & 7) >> 1) == i ? ((n & 1) ? 0xffff0000u : 0x0000ffffu) : 0u;
  float l = 0.f;
  f32x4 o[2] = {f32x4{0.f, 0.f, 0.f, 0.f}, f32x4{0.f, 0.f, 0.f, 0.f}};
#pragma unroll
  for (int t = 0; t < 2; ++t)
#pragma unroll
    for (int r = 0; r < 4; ++r) {
      const float p = __builtin_amdgcn_exp2f(s[t][r] - m);
      const unsigned pb = f2bf(p);
      l += bf2f(pb);
      const unsigned pp = pb | (pb << 16);
      const u32x4 bfrag = {pp & msk[0], pp & msk[1], pp & msk[2], pp & msk[3]};
      o[0] = mfma16(vf[t][r][0], bfrag, o[0]);
      o[1] = mfma16(vf[t][r][1], bfrag, o[1]);
    }
  l += __shfl_xor(l, 16);
  l += __shfl_xor(l, 32);
#pragma unroll
  for (int hf = 0; hf < 2; ++hf)
#pragma unroll
    for (int r = 0; r < 4; ++r) so[wave][(n >> 3) * 256 + hf * 128 + 8 * (4 * g + r) + (n & 7)] = o[hf][r];
  if (lane == 0 || lane == 8) { sml[wave][(n >> 3) * 2] = m; sml[wave][(n >> 3) * 2 + 1] = l; }
  __syncthreads();
  const int hh = tid >> 8;
  float M = -INFINITY;
#pragma unroll
  for (int w = 0; w < 8; ++w) M = fmaxf(M, sml[w][hh * 2]);
  float L = 0.f, acc = 0.f;
#pragma unroll
  for (int w = 0; w < 8; ++w) { const float sc = __builtin_amdgcn_exp2f(sml[w][hh * 2] - M); L += sc * sml[w][hh * 2 + 1]; acc += sc * so[w][tid]; }
  const int grp = (b * HKV + kvh) * NSPLIT + sp;
  po[grp * 512 + tid] = acc;
  if ((tid & 255) == 0) { pml[grp * 4 + hh * 2] = M; pml[grp * 4 + hh * 2 + 1] = L; }
}

__global__ void __launch_bounds__(448) k_out(const float* po, const float* pml, const float* h, const u16* wo, const u16* wd, float* ps, float* pd) {
  __shared__ float so[64], sh[28];
  const int wg = blockIdx.x, tid = threadIdx.x;
  const int kvh = wg & 7, b = wg >> 6, sp = (wg >> 3) & 7, j = kvh * 8 + sp;
  if (tid < 64) {
    const int hh = sp >> 2, g0 = (b * HKV + kvh) * NSPLIT;
    float M = -INFINITY;
    for (int s = 0; s < 8; ++s) M = fmaxf(M, pml[(g0 + s) * 4 + hh * 2]);
    float L = 0.f, acc = 0.f;
    for (int s = 0; s < 8; ++s) { const float sc = __builtin_amdgcn_exp2f(pml[(g0 + s) * 4 + hh * 2] - M); L += sc * pml[(g0 + s) * 4 + hh * 2 + 1]; acc += sc * po[(g0 + s) * 512 + sp * 64 + tid]; }
    so[tid] = rbf(acc / L);
  } else if (tid < 64 + 28) sh[tid - 64] = h[b * F + j * 28 + tid - 64];
  __syncthreads();
  float a = 0.f, d = 0.f;
#pragma unroll 16
  for (int r = 0; r < 64; ++r) a += so[r] * bf2f(wo[(size_t)(j * 64 + r) * D + tid]);
#pragma unroll 14
  for (int r = 0; r < 28; ++r) d += sh[r] * bf2f(wd[(size_t)(j * 28 + r) * D + tid]);
  ps[(b * 64 + j) * D + tid] = a;
  pd[(b * 64 + j) * D + tid] = d;
}

__global__ void __launch_bounds__(64) k_fin(const u16* x, const float* ps, const float* pd, u16* out) {
  const int i = blockIdx.x * 64 + threadIdx.x, b = i / D, c = i % D;
  float s = 0.f, d = 0.f;
#pragma unroll 16
  for (int j = 0; j < 64; ++j) { s += ps[(b * 64 + j) * D + c]; d += pd[(b * 64 + j) * D + c]; }
  s = rbf(s); d = rbf(d);
  const float g = rbf(d / (1.f + __expf(-d)));
  out[i] = f2bf(bf2f(x[i]) + rbf(s * g));
}

extern "C" void kernel_launch(void* const* d_in, const int* in_sizes, int n_in, void* d_out, int out_size, void* d_ws, size_t ws_size, hipStream_t stream) {
  const u16* x = (const u16*)d_in[0]; const u16* kc = (const u16*)d_in[1]; const u16* vc = (const u16*)d_in[2];
  const u16* wq = (const u16*)d_in[3]; const u16* wo = (const u16*)d_in[4];
  const u16* wg = (const u16*)d_in[6]; const u16* wu = (const u16*)d_in[7]; const u16* wd = (const u16*)d_in[8];
  char* ws = (char*)d_ws;
  u16* q = (u16*)ws; float* h = (float*)(ws + 65536); float* pml = (float*)(ws + 131072);
  float* po = (float*)(ws + (1 << 20)); float* ps = (float*)(ws + (2 << 20)); float* pd = (float*)(ws + (3 << 20));
  k_proj<<<92, 512, 0, stream>>>(x, wq, wg, wu, q, h);
  k_attn<<<256, 512, 0, stream>>>(q, kc, vc, po, pml);
  k_out<<<256, 448, 0, stream>>>(po, pml, h, wo, wd, ps, pd);
  k_fin<<<28, 64, 0, stream>>>(x, ps, pd, (u16*)d_out);
}
```

```cpp
#include <hip/hip_runtime.h>
#include <stdint.h>

typedef unsigned u32;
typedef unsigned short u16;
typedef unsigned long long u64;
typedef u32 u32x2 __attribute__((ext_vector_type(2)));
typedef u32 u32x4 __attribute__((ext_vector_type(4)));
typedef u64 u64x2 __attribute__((ext_vector_type(2)));
typedef u64 u64x8 __attribute__((ext_vector_type(8)));
typedef float f32x2 __attribute__((ext_vector_type(2)));
typedef float f32x4 __attribute__((ext_vector_type(4)));
typedef __bf16 bf16x2 __attribute__((ext_vector_type(2)));
typedef __bf16 bf16x8 __attribute__((ext_vector_type(8)));
typedef short s16x4 __attribute__((ext_vector_type(4)));
typedef __amdgpu_buffer_rsrc_t rsrc_t;
#define LAS __attribute__((address_space(3)))

constexpr int B = 4, D = 448, HQ = 16, HKV = 8, SK = 8192, DH = 256, W = 2048, F = 1792;
constexpr int NT = 2, SC1 = 16;
constexpr u32 OOB = 0x40000000u;
constexpr u32 SPIN_CAP = 1u << 17;
constexpr float LOG2E = 1.4426950408889634f;

constexpr u32 P1_ROW = (HQ * DH + 2 * F + 64) * 4;
constexpr u32 OB_RB = 32 * 512 + 512;
constexpr u32 BOX = 32 * 128 + 128;
constexpr u32 WS_EPOCH = 0;
constexpr u32 WS_LSE = 4096;
constexpr u32 WS_P1 = WS_LSE + HQ * 32 * 128;
constexpr u32 WS_OB = WS_P1 + 2 * B * P1_ROW;
constexpr u32 WS_SB = WS_OB + HKV * 4 * OB_RB;
constexpr u32 WS_END = WS_SB + 112 * BOX;

constexpr int BLK = 1408;
constexpr int L_CNT = 0;
constexpr int L_ML = 64;
constexpr int L_WT = L_ML + 256;
constexpr int L_QS = L_WT + 128;
constexpr int L_HS = L_QS + 2 * 576;
constexpr int L_XN = L_HS + 4 * 192;
constexpr int L_OG = L_XN + 4 * 960;
constexpr int L_SO = L_OG + 32 * 272;
constexpr int L_WD = L_SO + 16 * 2048;
constexpr int L_W1 = L_WD + 8 * BLK;
constexpr int L_WL = L_W1 + 28 * BLK;
constexpr int L_END = L_WL + 16 * BLK;
enum { C_STAGE = 0, C_Q = 4, C_ML = 8, C_OLD = 12, C_WO = 16, C_TAG = 28 };

struct Args {
  const u16 *kc, *vc, *x, *wq, *wgate, *wup, *wdown, *wo;
  u16* out;
  char* ws;
};

__device__ __forceinline__ float asf(u32 x) { return __uint_as_float(x); }
__device__ __forceinline__ u32 asu(float x) { return __float_as_uint(x); }
__device__ __forceinline__ float bflo(u32 x) { return asf(x << 16); }
__device__ __forceinline__ float bfhi(u32 x) { return asf(x & 0xffff0000u); }
__device__ __forceinline__ float rbf(float x) { return (float)(__bf16)x; }
__device__ __forceinline__ u32 pack2(float lo, float hi) { bf16x2 v = {(__bf16)lo, (__bf16)hi}; return __builtin_bit_cast(u32, v); }
__device__ __forceinline__ rsrc_t mkrsrc(const void* p, u32 bytes) { return __builtin_amdgcn_make_buffer_rsrc((void*)p, 0, bytes, 0x00020000); }
template <int AUX> __device__ __forceinline__ u32x4 ld128(rsrc_t r, u32 voff, u32 soff) { return __builtin_amdgcn_raw_buffer_load_b128(r, voff, soff, AUX); }
template <int AUX> __device__ __forceinline__ u32x2 ld64(rsrc_t r, u32 voff, u32 soff) { return __builtin_amdgcn_raw_buffer_load_b64(r, voff, soff, AUX); }
__device__ __forceinline__ bool none(u32 x) { return __builtin_amdgcn_ballot_w64(x != 0) == 0; }

__device__ __forceinline__ void put(rsrc_t r, u32 voff, u32 soff, u32 val, u32 tag) { __builtin_amdgcn_raw_buffer_store_b64(u32x2{val, tag}, r, voff, soff, SC1); }
__device__ __forceinline__ u32 bad(u32x4 v, u32 tag) { return (v[1] ^ tag) | (v[3] ^ tag); }
__device__ __forceinline__ float rn24(float v) { return asf(asu(v) + 0x80u); }
__device__ __forceinline__ u32 rec_word(float v, float v3, int i) { return __builtin_amdgcn_perm(asu(v), asu(v3), 0x07060503u - i); }
__device__ __forceinline__ u32 shl1_add(u32 a, u32 b) { u32 r; asm("v_lshl_add_u32 %0, %1, 1, %2" : "=v"(r) : "v"(a), "v"(b)); return r; }
__device__ __forceinline__ u32 rec_check(u32 w0, u32 w1, u32 w2, u32 tag) { return shl1_add(w1, w0) + w2 + tag; }
__device__ __forceinline__ u32 rec_bad(u32x4 v, u32 tag) { return rec_check(v[0], v[1], v[2], tag) ^ v[3]; }
__device__ __forceinline__ float rec_v(u32 w) { return asf(w & 0xffffff00u); }
__device__ __forceinline__ float rec_v3(u32x4 v) { return asf(__builtin_amdgcn_perm(__builtin_amdgcn_perm(v[0], v[1], 0x0400000cu), v[2], 0x0706000cu)); }

__device__ __forceinline__ f32x4 mfma16(u32x4 a, u32x4 b, f32x4 c) {
  return __builtin_amdgcn_mfma_f32_16x16x32_bf16(__builtin_bit_cast(bf16x8, a), __builtin_bit_cast(bf16x8, b), c, 0, 0, 0);
}
template <int CTRL> __device__ __forceinline__ float dpp(float x) {
  return __builtin_bit_cast(float, __builtin_amdgcn_mov_dpp(__builtin_bit_cast(int, x), CTRL, 0xf, 0xf, true));
}
constexpr int XOR1 = 0xB1, XOR2 = 0x4E, XOR7 = 0x141, XOR8 = 0x128, ROR4 = 0x124, BCAST = 0x150;
__device__ __forceinline__ float vmax(float a, float b) { float r; asm("v_max_f32 %0, %1, %2" : "=v"(r) : "v"(a), "v"(b)); return r; }
__device__ __forceinline__ float fold32(float lo, float hi) { const auto s = __builtin_amdgcn_permlane32_swap(asu(lo), asu(hi), false, false); return asf(s[0]) + asf(s[1]); }
__device__ __forceinline__ float fold16(float ev, float od) { const auto s = __builtin_amdgcn_permlane16_swap(asu(ev), asu(od), false, false); return asf(s[0]) + asf(s[1]); }
__device__ __forceinline__ float rows_sum(float x) { x = fold16(x, x); return fold32(x, x); }
__device__ __forceinline__ float rows_max(float x) {
  const auto s = __builtin_amdgcn_permlane16_swap(asu(x), asu(x), false, false);
  x = vmax(asf(s[0]), asf(s[1]));
  const auto t = __builtin_amdgcn_permlane32_swap(asu(x), asu(x), false, false);
  return vmax(asf(t[0]), asf(t[1]));
}
__device__ __forceinline__ float row8_sum(float x) { x += dpp<XOR1>(x); x += dpp<XOR2>(x); return x + dpp<XOR7>(x); }
__device__ __forceinline__ float row16_sum(float x) { x = row8_sum(x); return x + dpp<XOR8>(x); }
__device__ __forceinline__ float row8_max(float x) {
  asm("s_nop 1\n\tv_max_f32_dpp %0, %0, %0 quad_perm:[1,0,3,2] row_mask:0xf bank_mask:0xf\n\t"
      "s_nop 1\n\tv_max_f32_dpp %0, %0, %0 quad_perm:[2,3,0,1] row_mask:0xf bank_mask:0xf\n\t"
      "s_nop 1\n\tv_max_f32_dpp %0, %0, %0 row_half_mirror row_mask:0xf bank_mask:0xf\n\ts_nop 1" : "+v"(x));
  return x;
}
__device__ __forceinline__ float row16_max(float x) {
  x = row8_max(x);
  asm("s_nop 1\n\tv_max_f32_dpp %0, %0, %0 row_ror:8 row_mask:0xf bank_mask:0xf\n\ts_nop 1" : "+v"(x));
  return x;
}
__device__ __forceinline__ float wave_sum(float x) { return rows_sum(row16_sum(x)); }

#define PIN(x) asm volatile("" : "+v"(x))
#define SCHED() __builtin_amdgcn_sched_barrier(0)
#define POLL(mismatch) if (none(mismatch) || --cap == 0) break; asm volatile("" ::: "memory");
__device__ __forceinline__ void wg_barrier() { SCHED(); asm volatile("s_waitcnt lgkmcnt(0)\n\ts_barrier" ::: "memory"); SCHED(); }
template <int OFF> __device__ __forceinline__ void lds_signal(u32 base) {
  const u32 one = 1;
  asm volatile("s_waitcnt lgkmcnt(0)\n\ts_mov_b64 exec, 1\n\tds_add_u32 %0, %1 offset:%2\n\ts_mov_b64 exec, -1" :: "v"(base), "v"(one), "i"(OFF) : "memory");
}
template <int OFF, int WANT, int SLEEP> __device__ __forceinline__ void lds_wait(u32 base) {
  u32 v, s, cap = SPIN_CAP * 8;
  asm volatile("1:\n\tds_read_b32 %0, %3 offset:%4\n\ts_waitcnt lgkmcnt(0)\n\tv_readfirstlane_b32 %1, %0\n\ts_cmp_ge_u32 %1, %5\n\ts_cbranch_scc1 2f\n\t"
               "s_sleep %6\n\ts_sub_u32 %2, %2, 1\n\ts_cbranch_scc0 1b\n2:"
               : "=&v"(v), "=&s"(s), "+s"(cap) : "v"(base), "i"(OFF), "i"(WANT), "i"(SLEEP) : "memory", "scc");
}
__device__ __forceinline__ u32x4 tr_frag(LAS char* p) {
  const s16x4 a = __builtin_amdgcn_ds_read_tr16_b64_v4i16((LAS s16x4*)p);
  const s16x4 b = __builtin_amdgcn_ds_read_tr16_b64_v4i16((LAS s16x4*)(p + 4 * 160));
  const u32x2 x = __builtin_bit_cast(u32x2, a), y = __builtin_bit_cast(u32x2, b);
  return u32x4{x[0], x[1], y[0], y[1]};
}
constexpr u32 kv_off(int i) { return (i & 1) * 256 + (i & 2) * 1024 + (i & 4) * 128; }

__global__ void __attribute__((aligned(4096))) __launch_bounds__(1024, 4) fused_decode(const Args) {
  u64x8 ka;
  u64x2 kb;
  asm volatile("s_load_dwordx16 %0, %2, 0x0\n\ts_load_dwordx4 %1, %2, 0x40" : "=&s"(ka), "=&s"(kb) : "s"(__builtin_amdgcn_kernarg_segment_ptr()));
  SCHED();
  __shared__ __attribute__((aligned(16))) char smem[L_END];
  LAS char* lds = (LAS char*)smem;
  const u32 cbase = (u32)(uintptr_t)(lds + L_CNT);
  __builtin_amdgcn_s_setprio(2);
  const int wg = blockIdx.x, tid = threadIdx.x, lane = tid & 63;
  const int wave = __builtin_amdgcn_readfirstlane(tid >> 6), u = wave & 3;
  {
    const u32 zero = 0, addr = cbase + (tid & 7) * 4;
    asm volatile("s_cmp_lt_u32 %2, 4\n\ts_cbranch_scc1 1f\n\ts_cmp_lt_u32 %2, 15\n\ts_cbranch_scc1 2f\n\tds_write_b32 %0, %1\n\ts_waitcnt lgkmcnt(0)\n2:\n\ts_barrier\n1:"
                 :: "v"(addr), "v"(zero), "s"(wave) : "memory", "scc");
    SCHED();
  }
  const int kvh = wg & 7, r = wg >> 3, b = r >> 3, sp = r & 7;
  const int n = lane & 15, g = lane >> 4;
  const int rbc = (r * 37) >> 8, cb = r - rbc * 7, RB = ((kvh + 1) & 7) * 4 + rbc;
  const bool tail_on = r < 28, ffn_on = tail_on && RB < 28;
  const int odd = kvh & 1, gq = kvh >> 1, t1 = r >> 1, hi = (r + 4) >> 5, iq = odd | hi;
  const bool svc = wave < 4, isq = iq, on = !(odd & hi);
  const int half = r & 1, mat = t1 & 1;
  const int cq = odd * (t1 + ((t1 + 13) >> 4) + ((t1 + 6) >> 4)) + (1 - odd) * (3 + 8 * mat);
  const int cblk = iq * (gq * 16 + cq) + (1 - iq) * (gq * 7 + (r >> 2));
  u32 ld = F * 2 + iq * (HQ * DH * 2 - F * 2);
  u32 slab = ((b * HKV + kvh) * SK + (SK - W)) * DH * 2;
  u32 tile = ((wave >> 1) * 256 + sp * 32 + (u & 1) * 2) * 512;
  u32 wso = (half * 224 + wave * 56) * ld + cblk * 128, xso = wave * (D * 2);
  u32 wnum = on ? D * ld : 0;
  u32 wvo = (lane >> 3) * ld + (lane & 7) * 16, xvo = lane < 56 ? lane * 16 : OOB;
  u32 kvo = (n >> 2) * 4096 + g * 64 + (n & 3) * 16;
  asm volatile("" : "+s"(ld), "+s"(slab), "+s"(tile), "+s"(wso), "+s"(xso), "+s"(wnum));
  asm volatile("" : "+v"(wvo), "+v"(xvo), "+v"(kvo));
  SCHED();
  asm volatile("s_waitcnt lgkmcnt(0)" : "+s"(ka), "+s"(kb));
  SCHED();
  const Args a = {(const u16*)ka[0], (const u16*)ka[1], (const u16*)ka[2], (const u16*)ka[3], (const u16*)ka[4],
                  (const u16*)ka[5], (const u16*)ka[6], (const u16*)ka[7], (u16*)kb[0], (char*)kb[1]};
  const rsrc_t wsr = mkrsrc(a.ws, WS_END);
  const rsrc_t kr = mkrsrc((const char*)a.kc + slab, W * DH * 2), vr = mkrsrc((const char*)a.vc + slab, W * DH * 2);
  const u32 stage_off = (lane >> 3) * 160 + (lane & 7) * 16;
  const u32 tr_off = g * BLK + (n >> 2) * 160 + (n & 3) * 8 + u * 32;
  u32 tag = 0, cap = SPIN_CAP;

  u32x4 xv, w1[7];
  u32 ep;
  if (svc) {
    xv = ld128<0>(mkrsrc(a.x, B * D * 2), xvo, xso);
    ep = __builtin_amdgcn_raw_buffer_load_b32(wsr, 0, WS_EPOCH, SC1);
    const rsrc_t wr = mkrsrc(isq ? a.wq : (mat ? a.wup : a.wgate), wnum);
#pragma unroll
    for (int i = 0; i < 7; ++i) w1[i] = ld128<NT>(wr, wvo, wso + i * 8 * ld);
    wg_barrier();
  }
  u32x4 kf[8], vf[8];
#pragma unroll
  for (int i = 0; i < 8; ++i) { kf[i] = ld128<NT>(kr, kvo + kv_off(i), tile); SCHED(); }

  if (svc) {
    __builtin_amdgcn_s_setprio(3);
    float v[8];
#pragma unroll
    for (int i = 0; i < 4; ++i) { v[2 * i] = bflo(xv[i]); v[2 * i + 1] = bfhi(xv[i]); }
    const float mean = wave_sum(((v[0] + v[1]) + (v[2] + v[3])) + ((v[4] + v[5]) + (v[6] + v[7]))) * (1.f / D);
    float q2 = 0.f;
#pragma unroll
    for (int i = 0; i < 8; ++i) q2 += (v[i] - mean) * (v[i] - mean);
    const float var = wave_sum(lane < 56 ? q2 : 0.f) * (1.f / D);
    const float inv = __builtin_amdgcn_rcpf(rbf(__builtin_amdgcn_sqrtf(rbf(rbf(var) + 1e-5f)))), mb = rbf(mean);
    u32x4 xn;
#pragma unroll
    for (int i = 0; i < 4; ++i) xn[i] = pack2(rbf(v[2 * i] - mb) * inv, rbf(v[2 * i + 1] - mb) * inv);
    if (lane < 56) *(LAS u32x4*)(lds + L_XN + wave * 960 + lane * 16) = xn;
    tag = (u32)__builtin_amdgcn_readfirstlane(ep) + 0x9E3779B9u;
    if (tid == 0) *(LAS u32*)(lds + L_CNT + C_TAG) = tag;
#pragma unroll
    for (int i = 0; i < 7; ++i) *(LAS u32x4*)(lds + L_W1 + (wave * 7 + i) * BLK + stage_off) = w1[i];
    lds_signal<C_STAGE>(cbase);
    lds_wait<C_STAGE, 4, 1>(cbase);
    f32x4 acc = {0.f, 0.f, 0.f, 0.f};
#pragma unroll
    for (int ks = 0; ks < 7; ++ks) {
      const u32x4 xa = *(LAS u32x4*)(lds + L_XN + (n & 3) * 960 + half * 448 + ks * 64 + g * 16);
      acc = mfma16(tr_frag(lds + L_W1 + ks * 4 * BLK + tr_off), xa, acc);
    }
    const u32 pcol = isq ? cblk * 64 : HQ * DH + mat * F + cblk * 64;
    const float v3 = rn24(acc[3]);
    const u32 w0 = rec_word(rn24(acc[0]), v3, 0), w1_ = rec_word(rn24(acc[1]), v3, 1), w2 = rec_word(rn24(acc[2]), v3, 2);
    if (on && n < 4)
      __builtin_amdgcn_raw_buffer_store_b128(u32x4{w0, w1_, w2, rec_check(w0, w1_, w2, tag)}, wsr, n * P1_ROW + g * 16,
                                             WS_P1 + half * B * P1_ROW + (pcol + wave * 16) * 4, SC1);
  }
  SCHED();
  const u32 qso = WS_P1 + b * P1_ROW + (kvh * 512 + u * 128) * 4, qvo = (lane >> 5) * (B * P1_ROW) + (lane & 31) * 16;
  const u32 hso = ffn_on ? WS_P1 + u * P1_ROW + (HQ * DH + RB * 64) * 4 : qso;
  const u32 hvo = (lane >> 5) * (B * P1_ROW) + (g & 1) * (ffn_on ? F * 4 : 256) + n * 16;
  const u32 vvo = g * 4096 + n * 16;
  u32x4 q0, h0;
  __builtin_amdgcn_s_setprio(1);
  if (svc) {
    q0 = ld128<SC1>(wsr, qvo, qso);
    h0 = ld128<SC1>(wsr, hvo, hso);
  }
  SCHED();
#pragma unroll
  for (int i = 0; i < 8; ++i) { vf[i] = ld128<NT>(vr, vvo + kv_off(i), tile); SCHED(); }
  __builtin_amdgcn_s_setprio(3);

  if (svc) {
    if (!none(rec_bad(q0, tag) | rec_bad(h0, tag))) {
#pragma nounroll
      for (;;) {
        q0 = ld128<SC1>(wsr, qvo, qso);
        h0 = ld128<SC1>(wsr, hvo, hso);
        POLL(rec_bad(q0, tag) | rec_bad(h0, tag))
      }
    }
    *(LAS u32*)(lds + L_QS + (wave >> 1) * 576 + (wave & 1) * 256 + (lane & 31) * 8 + (lane >> 5) * 4) =
        pack2(fold32(rec_v(q0[0]), rec_v(q0[2])), fold32(rec_v(q0[1]), rec_v3(q0)));
    const u32 gu = pack2(fold32(rec_v(h0[0]), rec_v(h0[2])), fold32(rec_v(h0[1]), rec_v3(h0)));
    const auto sw = __builtin_amdgcn_permlane16_swap(gu, gu, false, false);
    const float g0 = bflo(sw[0]), g1 = bfhi(sw[0]);
    const u32 hh = pack2((g0 > 0.f ? g0 : 0.f) * bflo(sw[1]), (g1 > 0.f ? g1 : 0.f) * bfhi(sw[1]));
    if (!(g & 1)) *(LAS u32*)(lds + L_HS + wave * 192 + n * 8 + (lane >> 5) * 4) = ffn_on ? hh : 0u;
    lds_signal<C_Q>(cbase);
  }
  lds_wait<C_Q, 4, 4>(cbase);

  const u32x4 qa = *(LAS u32x4*)(lds + L_QS + (n >> 3) * 576 + g * 64 + (n & 3) * 16);
  const u32x4 qb = *(LAS u32x4*)(lds + L_QS + (n >> 3) * 576 + g * 64 + (n & 3) * 16 + 256);
  tag = __builtin_amdgcn_readfirstlane(*(LAS u32*)(lds + L_CNT + C_TAG));
  float sc[4];
#pragma unroll
  for (int i = 0; i < 4; ++i) {
    const f32x4 t = mfma16(kf[2 * i + 1], qb, mfma16(kf[2 * i], qa, f32x4{0.f, 0.f, 0.f, 0.f}));
    float s = dpp<0x00>(t[0]);
    s += dpp<0x55>(t[1]); PIN(s);
    s += dpp<0xAA>(t[2]); PIN(s);
    s += dpp<0xFF>(t[3]);
    sc[i] = s;
  }
  {
    const u32 s01 = pack2(sc[0], sc[1]), s23 = pack2(sc[2], sc[3]);
    sc[0] = bflo(s01); sc[1] = bfhi(s01); sc[2] = bflo(s23); sc[3] = bfhi(s23);
  }
  constexpr float C2 = 0.0625f * LOG2E;
  const float m = rows_max(vmax(vmax(sc[0], sc[1]), vmax(sc[2], sc[3]))) * C2;
  u32 pp[4];
#pragma unroll
  for (int i = 0; i < 4; ++i) { const float p = __builtin_amdgcn_exp2f(sc[i] * C2 - m); pp[i] = pack2(p, p); PIN(pp[i]); }
  const float l = rows_sum((bfhi(pp[0]) + bfhi(pp[1])) + (bfhi(pp[2]) + bfhi(pp[3])));
  if ((lane & 55) == 0) *(LAS f32x2*)(lds + L_ML + (lane >> 3) * 128 + wave * 8) = f32x2{m, l};
  lds_signal<C_ML>(cbase);
  u32x4 bf[4];
  {
    const u32 hm = (n & 1) ? 0xffff0000u : 0x0000ffffu, q = (n >> 1) & 3;
    const u32x4 msk = {q == 0 ? hm : 0u, q == 1 ? hm : 0u, q == 2 ? hm : 0u, q == 3 ? hm : 0u};
#pragma unroll
    for (int i = 0; i < 4; ++i) { bf[i] = msk & pp[i]; PIN(bf[i]); }
  }
  u32 so_off = wave * 2048 + (n >> 3) * 1024 + (g * 8 + (n & 7)) * 16;
  PIN(so_off);
  SCHED();
  f32x4 o0 = {0.f, 0.f, 0.f, 0.f}, o1 = {0.f, 0.f, 0.f, 0.f};
#pragma unroll
  for (int i = 0; i < 4; ++i) {
    o0 = mfma16(vf[2 * i], bf[i], o0);
    o1 = mfma16(vf[2 * i + 1], bf[i], o1);
  }
  *(LAS f32x4*)(lds + L_SO + so_off) = o0;
  *(LAS f32x4*)(lds + L_SO + so_off + 512) = o1;
  {
    const u32 one = 1;
    SCHED();
    asm volatile("s_cmp_lt_u32 %2, 12\n\ts_waitcnt lgkmcnt(0)\n\ts_cbranch_scc1 1f\n\ts_barrier\n\ts_branch 2f\n1:\n\t"
                 "s_mov_b64 exec, 1\n\tds_add_u32 %0, %1 offset:%3\n\ts_mov_b64 exec, -1\n2:" :: "v"(cbase), "v"(one), "s"(wave), "i"(C_OLD) : "memory", "scc");
    SCHED();
  }

  if (wave >= 8) {
    u32 so = WS_OB + RB * OB_RB + (wave - 8) * 2048;
    u32 og_off = ((wave - 8) * 4 + (lane >> 5)) * 272 + (lane & 31) * 8, ovo = lane * 16;
    asm volatile("" : "+v"(og_off), "+v"(ovo));
    asm volatile("" : "+s"(so));
    if (wave < 12) wg_barrier();
    if (tail_on) {
      __builtin_amdgcn_s_sleep(3);
      u32x4 t0, t1;
#pragma nounroll
      for (;;) {
        t0 = ld128<SC1>(wsr, ovo, so);
        t1 = ld128<SC1>(wsr, ovo + 1024, so);
        POLL(bad(t0, tag) | bad(t1, tag))
      }
      LAS u32* og = (LAS u32*)(lds + L_OG + og_off);
      asm volatile("ds_write2_b32 %0, %1, %2 offset1:1\n\tds_write2_b32 %0, %3, %4 offset0:136 offset1:137"
                   :: "v"(og), "v"(t0[0]), "v"(t0[2]), "v"(t1[0]), "v"(t1[2]) : "memory");
      wg_barrier();
    }
  } else if (wave >= 4) {
    const bool fin = wave == 4 && tail_on;
    u32 oi = ((RB >> 3) * D + cb * 64 + (RB & 7) * 8 + (lane & 3) * 2 + (g & 1)) * 2;
    if (!fin || (lane & 44)) oi = OOB;
    const u32 xraw = __builtin_amdgcn_raw_buffer_load_b16(mkrsrc(a.x, B * D * 2), oi, 0, 0);
    lds_wait<C_ML, 16, 1>(cbase);
    const f32x2 ml = *(LAS f32x2*)(lds + L_ML + (u >> 1) * 128 + n * 8);
    const float M = row16_max(ml[0]);
    const float e = __builtin_amdgcn_exp2f(ml[0] - M);
    const float L = row16_sum(e * ml[1]);
    const float lse = M + __builtin_amdgcn_logf(L);
    if (!(u & 1)) put(wsr, lane < 16 ? lane * 8 : OOB, WS_LSE + ((kvh * 2 + (u >> 1)) * 32 + r) * 128, asu(lse), tag);
    const float c = e * __builtin_amdgcn_rcpf(L);
    lds_wait<C_OLD, 12, 1>(cbase);
    LAS char* sp_ = lds + L_SO + u * 512 + lane * 8;
    f32x2 ov[12];
#pragma unroll
    for (int i = 0; i < 12; ++i) ov[i] = *(LAS f32x2*)(sp_ + i * 2048);
    SCHED();
    float a0 = 0.f, a1 = 0.f;
#define MRG(i) { const float ci = dpp<BCAST + i>(c); a0 = fmaf(ci, ov[i][0], a0); a1 = fmaf(ci, ov[i][1], a1); }
    MRG(0) MRG(1) MRG(2) MRG(3) MRG(4) MRG(5) MRG(6) MRG(7) MRG(8) MRG(9) MRG(10) MRG(11)
#undef MRG
    float c12 = dpp<BCAST + 12>(c), c13 = dpp<BCAST + 13>(c), c14 = dpp<BCAST + 14>(c), c15 = dpp<BCAST + 15>(c);
    u32 ob_so = WS_OB + (kvh * 4 + u) * OB_RB + r * 512, ob_vo = lane * 8, tagv = tag;
    asm volatile("" : "+v"(a0), "+v"(a1), "+v"(c12), "+v"(c13), "+v"(c14), "+v"(c15), "+v"(ob_vo), "+v"(tagv));
    asm volatile("" : "+s"(ob_so));
    wg_barrier();
    {
      const f32x2 y0 = *(LAS f32x2*)(sp_ + 12 * 2048), y1 = *(LAS f32x2*)(sp_ + 13 * 2048);
      const f32x2 y2 = *(LAS f32x2*)(sp_ + 14 * 2048), y3 = *(LAS f32x2*)(sp_ + 15 * 2048);
      a0 = fmaf(c15, y3[0], fmaf(c14, y2[0], fmaf(c13, y1[0], fmaf(c12, y0[0], a0))));
      a1 = fmaf(c15, y3[1], fmaf(c14, y2[1], fmaf(c13, y1[1], fmaf(c12, y0[1], a1))));
      put(wsr, ob_vo, ob_so, pack2(a0, a1), tagv);
    }
    SCHED();
    if (tail_on) {
      if (wave == 5) {
        u32x2 lz;
#pragma nounroll
        for (;;) {
          lz = ld64<SC1>(wsr, (lane & 31) * 128, WS_LSE + (RB >> 1) * 32 * 128);
          POLL(lz[1] ^ tag)
        }
        const float z = asf(lz[0]);
        const float ez = __builtin_amdgcn_exp2f(z - row8_max(z));
        *(LAS float*)(lds + L_WT + (lane & 31) * 4) = ez * __builtin_amdgcn_rcpf(row8_sum(ez));
      }
      u32 box = WS_SB + (cb * 16 + (RB >> 1)) * BOX + (RB & 1) * 64, bvo = (lane >> 2) * 128 + (lane & 3) * 16;
      asm volatile("" : "+v"(bvo));
      asm volatile("" : "+s"(box));
      wg_barrier();
      if (fin) {
        float xres = bflo(xraw);
        PIN(xres);
        __builtin_amdgcn_s_sleep(18);
        u32x4 t0, t1;
#pragma nounroll
        for (;;) {
          t0 = ld128<SC1>(wsr, bvo, box);
          t1 = ld128<SC1>(wsr, bvo + 2048, box);
          POLL(rec_bad(t0, tag) | rec_bad(t1, tag))
        }
        const float s0 = asf(t0[0]) + asf(t1[0]), s1 = asf(t0[1]) + asf(t1[1]), d0 = asf(t0[2]) + asf(t1[2]), d1 = rec_v3(t0) + rec_v3(t1);
        float y = fold32(fold16(s0, s1), fold16(d0, d1));
        y += dpp<XOR8>(y);
        y += dpp<ROR4>(y);
        y = rbf(y);
        const auto sd = __builtin_amdgcn_permlane32_swap(asu(y), asu(y), false, false);
        const float dsum = asf(sd[1]);
        const float gate = rbf(dsum * __builtin_amdgcn_rcpf(1.f + __builtin_amdgcn_exp2f(dsum * -LOG2E)));
        __builtin_amdgcn_raw_buffer_store_b16((u16)pack2(xres + rbf(asf(sd[0]) * gate), 0.f), mkrsrc(a.out, B * D * 2), oi, 0, 0);
      }
    } else if (wg == 255 && wave == 5) {
#pragma nounroll
      for (;;) {
        __builtin_amdgcn_s_sleep(16);
        u32 x = 0;
#pragma unroll
        for (int i = 0; i < 4; ++i) x |= ld64<SC1>(wsr, (lane & 31) * 128 + (lane >> 5) * 8192, WS_LSE + i * 16384)[1] ^ tag;
        POLL(x)
      }
      if (lane == 0) __builtin_amdgcn_raw_buffer_store_b32(tag, wsr, 0, WS_EPOCH, SC1);
    }
  } else if (tail_on) {
    const rsrc_t wor = mkrsrc(a.wo, HQ * DH * D * 2), wdr = mkrsrc(a.wdown, ffn_on ? F * D * 2 : 0);
    u32 d_vo = (lane >> 3) * (D * 2) + (lane & 7) * 16, o_vo = (8 * ((lane >> 3) & 3) + (lane >> 5)) * (D * 2) + (lane & 7) * 16;
    u32 d_so = (RB * 64 + 16 * wave) * (D * 2) + cb * 128, o_so = (RB * 128 + 32 * wave) * (D * 2) + cb * 128;
    asm volatile("" : "+v"(d_vo), "+v"(o_vo));
    asm volatile("" : "+s"(d_so), "+s"(o_so));
    const int rb2 = wave >> 1, ch = wave & 1;
    u32 st_d = L_WD + wave * 2 * BLK + stage_off, st_o = L_WL + wave * 4 * BLK + stage_off;
    u32 tr_d = L_WD + g * BLK + (n >> 2) * 160 + (n & 3) * 8 + ch * 64, tr_o = tr_d + (L_WL - L_WD);
    u32 og_off = L_OG + (rb2 * 16 + n) * 272 + g * 16, wt_off = L_WT + rb2 * 64 + g * 16;
    u32 sb_vo = (n & 1) ? OOB : ((g >> 1) * 4 + (g & 1)) * BOX + n * 8, sb_so = WS_SB + (cb * 16 + rb2 * 8 + ch * 2) * BOX + RB * 128;
    u32x4 ha0 = *(LAS u32x4*)(lds + L_HS + (rb2 * 2 + (n >> 3)) * 192 + g * 16);
    u32x4 ha1 = *(LAS u32x4*)(lds + L_HS + (rb2 * 2 + (n >> 3)) * 192 + 64 + g * 16);
    asm volatile("" : "+v"(st_d), "+v"(st_o), "+v"(tr_d), "+v"(tr_o), "+v"(og_off), "+v"(wt_off), "+v"(sb_vo), "+v"(ha0), "+v"(ha1));
    asm volatile("" : "+s"(sb_so));
    SCHED();
    __builtin_amdgcn_s_setprio(0);
    lds_wait<C_OLD, 12, 2>(cbase);
    u32x4 wl[6];
#pragma unroll
    for (int i = 0; i < 2; ++i) wl[i] = ld128<NT>(wdr, d_vo, d_so + 8 * i * (D * 2));
#pragma unroll
    for (int i = 0; i < 4; ++i) wl[2 + i] = ld128<NT>(wor, o_vo + (i & 1) * 2 * (D * 2), o_so + (i & 2) * 2 * (D * 2));
    SCHED();
    __builtin_amdgcn_s_setprio(2);
    wg_barrier();
#pragma unroll
    for (int i = 0; i < 2; ++i) *(LAS u32x4*)(lds + st_d + i * BLK) = wl[i];
#pragma unroll
    for (int i = 0; i < 4; ++i) *(LAS u32x4*)(lds + st_o + i * BLK) = wl[2 + i];
    lds_signal<C_WO>(cbase);
    lds_wait<C_WO, 4, 0>(cbase);
    u32x4 bd[4], bw[8];
#pragma unroll
    for (int i = 0; i < 4; ++i) bd[i] = tr_frag(lds + tr_d + (i >> 1) * 4 * BLK + (i & 1) * 32);
#pragma unroll
    for (int i = 0; i < 8; ++i) bw[i] = tr_frag(lds + tr_o + (i >> 1) * 4 * BLK + (i & 1) * 32);
    SCHED();
    const f32x4 z4 = {0.f, 0.f, 0.f, 0.f};
    const f32x4 da0 = mfma16(ha1, bd[2], mfma16(ha0, bd[0], z4)), da1 = mfma16(ha1, bd[3], mfma16(ha0, bd[1], z4));
    const float dn = (g & 1) ? da1[0] : da0[0];
    float dn1 = dpp<XOR1>(dn);
    u32 w2 = rec_word(dn, dn1, 2), w2t = w2 + tag;
    asm volatile("" : "+v"(dn1), "+v"(w2), "+v"(w2t), "+v"(bw[0]), "+v"(bw[1]), "+v"(bw[2]), "+v"(bw[3]), "+v"(bw[4]), "+v"(bw[5]), "+v"(bw[6]), "+v"(bw[7]));
    wg_barrier();
    u32x4 oa[4];
#pragma unroll
    for (int ks = 0; ks < 4; ++ks) oa[ks] = *(LAS u32x4*)(lds + og_off + ks * 64);
    const f32x4 wt = *(LAS f32x4*)(lds + wt_off);
    SCHED();
    f32x4 a0 = z4, a1 = z4;
#pragma unroll
    for (int ks = 0; ks < 4; ++ks) {
      a0 = mfma16(oa[ks], bw[2 * ks], a0);
      a1 = mfma16(oa[ks], bw[2 * ks + 1], a1);
    }
    float s0 = a0[0] * wt[0];
    s0 = fmaf(a0[1], wt[1], s0); s0 = fmaf(a0[2], wt[2], s0); s0 = fmaf(a0[3], wt[3], s0);
    PIN(s0);
    float s1 = a1[0] * wt[0];
    s1 = fmaf(a1[1], wt[1], s1); s1 = fmaf(a1[2], wt[2], s1); s1 = fmaf(a1[3], wt[3], s1);
    const float sv = fold16(s0, s1);
    const u32 r0 = rec_word(sv, dn1, 0), r1 = rec_word(dpp<XOR1>(sv), dn1, 1);
    __builtin_amdgcn_raw_buffer_store_b128(u32x4{r0, r1, w2, shl1_add(r1, r0 + w2t)}, wsr, sb_vo, sb_so, SC1);
  }
}

extern "C" void kernel_launch(void* const* d_in, const int* in_sizes, int n_in, void* d_out, int out_size, void* d_ws, size_t ws_size,
                              hipStream_t stream) {
  Args a;
  a.kc = (const u16*)d_in[1];
  a.vc = (const u16*)d_in[2];
  a.x = (const u16*)d_in[0];
  a.wq = (const u16*)d_in[3];
  a.wgate = (const u16*)d_in[6];
  a.wup = (const u16*)d_in[7];
  a.wdown = (const u16*)d_in[8];
  a.wo = (const u16*)d_in[4];
  a.out = (u16*)d_out;
  a.ws = (char*)d_ws;
  fused_decode<<<256, 1024, 0, stream>>>(a);
}
```

```cpp
#include <hip/hip_runtime.h>
#include <stdint.h>

typedef unsigned u32;
typedef unsigned short u16;
typedef unsigned long long u64;
typedef u32 u32x2 __attribute__((ext_vector_type(2)));
typedef u32 u32x3 __attribute__((ext_vector_type(3)));
typedef u32 u32x4 __attribute__((ext_vector_type(4)));
typedef u64 u64x2 __attribute__((ext_vector_type(2)));
typedef u64 u64x8 __attribute__((ext_vector_type(8)));
typedef float f32x2 __attribute__((ext_vector_type(2)));
typedef float f32x4 __attribute__((ext_vector_type(4)));
typedef __bf16 bf16x2 __attribute__((ext_vector_type(2)));
typedef __bf16 bf16x8 __attribute__((ext_vector_type(8)));
typedef short s16x4 __attribute__((ext_vector_type(4)));
typedef __amdgpu_buffer_rsrc_t rsrc_t;
#define LAS __attribute__((address_space(3)))

constexpr int B = 4, D = 448, HQ = 16, HKV = 8, SK = 8192, DH = 256, W = 2048, F = 1792;
constexpr int NT = 2, SC1 = 16;
constexpr u32 OOB = 0x40000000u;
constexpr u32 SPIN_CAP = 1u << 17;
constexpr float LOG2E = 1.4426950408889634f;

constexpr u32 P1_ROW = (HQ * DH + 2 * F + 64) * 4;
constexpr u32 OB_RB = 33 * 384;
constexpr u32 BOX = 32 * 128 + 128;
constexpr u32 WS_EPOCH = 0;
constexpr u32 WS_LSE = 4096;
constexpr u32 WS_P1 = WS_LSE + HQ * 32 * 128;
constexpr u32 WS_OB = WS_P1 + 2 * B * P1_ROW;
constexpr u32 WS_SB = WS_OB + HKV * 4 * OB_RB;
constexpr u32 WS_END = WS_SB + 112 * BOX;

constexpr int BLK = 1408;
constexpr int L_CNT = 0;
constexpr int L_ML = 64;
constexpr int L_WT = L_ML + 256;
constexpr int L_QS = L_WT + 128;
constexpr int L_HS = L_QS + 2 * 576;
constexpr int L_XN = L_HS + 4 * 192;
constexpr int L_OG = L_XN + 4 * 960;
constexpr int L_SO = L_OG + 32 * 272;
constexpr int L_WD = L_SO + 16 * 2048;
constexpr int L_W1 = L_WD + 8 * BLK;
constexpr int L_WL = L_W1 + 28 * BLK;
constexpr int L_END = L_WL + 16 * BLK;
enum { C_STAGE = 0, C_Q = 4, C_ML = 8, C_OLD = 12, C_WO = 16, C_TAG = 28 };

struct Args {
  const u16 *kc, *vc, *x, *wq, *wgate, *wup, *wdown, *wo;
  u16* out;
  char* ws;
};

__device__ __forceinline__ float asf(u32 x) { return __uint_as_float(x); }
__device__ __forceinline__ u32 asu(float x) { return __float_as_uint(x); }
__device__ __forceinline__ float bflo(u32 x) { return asf(x << 16); }
__device__ __forceinline__ float bfhi(u32 x) { return asf(x & 0xffff0000u); }
__device__ __forceinline__ float rbf(float x) { return (float)(__bf16)x; }
__device__ __forceinline__ u32 pack2(float lo, float hi) { bf16x2 v = {(__bf16)lo, (__bf16)hi}; return __builtin_bit_cast(u32, v); }
__device__ __forceinline__ rsrc_t mkrsrc(const void* p, u32 bytes) { return __builtin_amdgcn_make_buffer_rsrc((void*)p, 0, bytes, 0x00020000); }
template <int AUX> __device__ __forceinline__ u32x4 ld128(rsrc_t r, u32 voff, u32 soff) { return __builtin_amdgcn_raw_buffer_load_b128(r, voff, soff, AUX); }
template <int AUX> __device__ __forceinline__ u32x2 ld64(rsrc_t r, u32 voff, u32 soff) { return __builtin_amdgcn_raw_buffer_load_b64(r, voff, soff, AUX); }
__device__ __forceinline__ bool none(u32 x) { return __builtin_amdgcn_ballot_w64(x != 0) == 0; }

__device__ __forceinline__ float rn24(float v) { return asf(asu(v) + 0x80u); }
__device__ __forceinline__ u32 rec_word(float v, float v3, int i) { return __builtin_amdgcn_perm(asu(v), asu(v3), 0x07060503u - i); }
__device__ __forceinline__ u32 rec_check(u32 w0, u32 w1, u32 w2, u32 tag) { return w0 + w1 + w2 + tag; }
__device__ __forceinline__ u32 rec_bad(u32x4 v, u32 tag) { return (v[0] + v[1] + tag) ^ (v[3] - v[2]); }
__device__ __forceinline__ float rec_v(u32 w) { return asf(w & 0xffffff00u); }
__device__ __forceinline__ float rec_v3(u32x4 v) { return asf(__builtin_amdgcn_perm(__builtin_amdgcn_perm(v[0], v[1], 0x0400000cu), v[2], 0x0706000cu)); }

__device__ __forceinline__ f32x4 mfma16(u32x4 a, u32x4 b, f32x4 c) {
  return __builtin_amdgcn_mfma_f32_16x16x32_bf16(__builtin_bit_cast(bf16x8, a), __builtin_bit_cast(bf16x8, b), c, 0, 0, 0);
}
template <int CTRL> __device__ __forceinline__ float dpp(float x) {
  return __builtin_bit_cast(float, __builtin_amdgcn_mov_dpp(__builtin_bit_cast(int, x), CTRL, 0xf, 0xf, true));
}
constexpr int XOR1 = 0xB1, XOR2 = 0x4E, XOR7 = 0x141, XOR8 = 0x128, ROR4 = 0x124, BCAST = 0x150;
__device__ __forceinline__ float vmax(float a, float b) { float r; asm("v_max_f32 %0, %1, %2" : "=v"(r) : "v"(a), "v"(b)); return r; }
__device__ __forceinline__ float fold32(float lo, float hi) { const auto s = __builtin_amdgcn_permlane32_swap(asu(lo), asu(hi), false, false); return asf(s[0]) + asf(s[1]); }
__device__ __forceinline__ float fold16(float ev, float od) { const auto s = __builtin_amdgcn_permlane16_swap(asu(ev), asu(od), false, false); return asf(s[0]) + asf(s[1]); }
__device__ __forceinline__ float rows_sum(float x) { x = fold16(x, x); return fold32(x, x); }
__device__ __forceinline__ float rows_max(float x) {
  const auto s = __builtin_amdgcn_permlane16_swap(asu(x), asu(x), false, false);
  x = vmax(asf(s[0]), asf(s[1]));
  const auto t = __builtin_amdgcn_permlane32_swap(asu(x), asu(x), false, false);
  return vmax(asf(t[0]), asf(t[1]));
}
__device__ __forceinline__ float row8_sum(float x) { x += dpp<XOR1>(x); x += dpp<XOR2>(x); return x + dpp<XOR7>(x); }
__device__ __forceinline__ float row16_sum(float x) { x = row8_sum(x); return x + dpp<XOR8>(x); }
__device__ __forceinline__ float row8_max(float x) {
  asm("s_nop 1\n\tv_max_f32_dpp %0, %0, %0 quad_perm:[1,0,3,2] row_mask:0xf bank_mask:0xf\n\t"
      "s_nop 1\n\tv_max_f32_dpp %0, %0, %0 quad_perm:[2,3,0,1] row_mask:0xf bank_mask:0xf\n\t"
      "s_nop 1\n\tv_max_f32_dpp %0, %0, %0 row_half_mirror row_mask:0xf bank_mask:0xf\n\ts_nop 1" : "+v"(x));
  return x;
}
__device__ __forceinline__ float row16_max(float x) {
  x = row8_max(x);
  asm("s_nop 1\n\tv_max_f32_dpp %0, %0, %0 row_ror:8 row_mask:0xf bank_mask:0xf\n\ts_nop 1" : "+v"(x));
  return x;
}
__device__ __forceinline__ float wave_sum(float x) { return rows_sum(row16_sum(x)); }

#define PIN(x) asm volatile("" : "+v"(x))
#define SCHED() __builtin_amdgcn_sched_barrier(0)
#define POLL_BEGIN() do { u32 lim = (u32)(--cap >> 31); asm volatile("" : "+s"(lim) :: "memory")
#define POLL_END(mismatch) more = __builtin_amdgcn_ballot_w64((mismatch) > lim); } while (more)
__device__ __forceinline__ void wg_barrier() { SCHED(); asm volatile("s_waitcnt lgkmcnt(0)\n\ts_barrier" ::: "memory"); SCHED(); }
template <int OFF> __device__ __forceinline__ void lds_signal(u32 base) {
  const u32 one = 1;
  asm volatile("s_waitcnt lgkmcnt(0)\n\ts_mov_b64 exec, 1\n\tds_add_u32 %0, %1 offset:%2\n\ts_mov_b64 exec, -1" :: "v"(base), "v"(one), "i"(OFF) : "memory");
}
template <int OFF, int WANT, int SLEEP> __device__ __forceinline__ void lds_wait(u32 base) {
  u32 v, s, cap = SPIN_CAP * 8;
  asm volatile("1:\n\tds_read_b32 %0, %3 offset:%4\n\ts_waitcnt lgkmcnt(0)\n\tv_readfirstlane_b32 %1, %0\n\ts_cmp_ge_u32 %1, %5\n\ts_cbranch_scc1 2f\n\t"
               "s_sleep %6\n\ts_sub_u32 %2, %2, 1\n\ts_cbranch_scc0 1b\n2:"
               : "=&v"(v), "=&s"(s), "+s"(cap) : "v"(base), "i"(OFF), "i"(WANT), "i"(SLEEP) : "memory", "scc");
}
__device__ __forceinline__ u32x4 tr_frag(LAS char* p) {
  const s16x4 a = __builtin_amdgcn_ds_read_tr16_b64_v4i16((LAS s16x4*)p);
  const s16x4 b = __builtin_amdgcn_ds_read_tr16_b64_v4i16((LAS s16x4*)(p + 4 * 160));
  const u32x2 x = __builtin_bit_cast(u32x2, a), y = __builtin_bit_cast(u32x2, b);
  return u32x4{x[0], x[1], y[0], y[1]};
}
constexpr u32 kv_off(int i) { return (i & 1) * 256 + (i & 2) * 1024 + (i & 4) * 128; }

__global__ void __attribute__((aligned(4096))) __launch_bounds__(1024, 4) fused_decode(const Args) {
  u64x8 ka;
  u64x2 kb;
  asm volatile("s_load_dwordx16 %0, %2, 0x0\n\ts_load_dwordx4 %1, %2, 0x40" : "=&s"(ka), "=&s"(kb) : "s"(__builtin_amdgcn_kernarg_segment_ptr()));
  SCHED();
  __shared__ __attribute__((aligned(16))) char smem[L_END];
  LAS char* lds = (LAS char*)smem;
  const u32 cbase = (u32)(uintptr_t)(lds + L_CNT);
  __builtin_amdgcn_s_setprio(2);
  const int wg = blockIdx.x, tid = threadIdx.x, lane = tid & 63;
  const int wave = __builtin_amdgcn_readfirstlane(tid >> 6), u = wave & 3;
  {
    const u32 zero = 0, addr = cbase + (tid & 7) * 4;
    asm volatile("s_cmp_lt_u32 %2, 4\n\ts_cbranch_scc1 1f\n\ts_cmp_lt_u32 %2, 15\n\ts_cbranch_scc1 2f\n\tds_write_b32 %0, %1\n\ts_waitcnt lgkmcnt(0)\n2:\n\ts_barrier\n1:"
                 :: "v"(addr), "v"(zero), "s"(wave) : "memory", "scc");
    SCHED();
  }
  const int kvh = wg & 7, r = wg >> 3, b = r >> 3, sp = r & 7;
  const int n = lane & 15, g = lane >> 4;
  const int rbc = (r * 37) >> 8, cb = r - rbc * 7, RB = ((kvh + 1) & 7) * 4 + rbc;
  const bool tail_on = r < 28, ffn_on = tail_on && RB < 28;
  const int odd = kvh & 1, gq = kvh >> 1, t1 = r >> 1, hi = (r + 4) >> 5, iq = odd | hi;
  const bool svc = wave < 4, isq = iq, on = !(odd & hi);
  const int half = r & 1, mat = t1 & 1;
  const int cq = odd * (t1 + ((t1 + 13) >> 4) + ((t1 + 6) >> 4)) + (1 - odd) * (3 + 8 * mat);
  const int cblk = iq * (gq * 16 + cq) + (1 - iq) * (gq * 7 + (r >> 2));
  u32 ld = F * 2 + iq * (HQ * DH * 2 - F * 2);
  u32 slab = ((b * HKV + kvh) * SK + (SK - W)) * DH * 2;
  u32 tile = ((wave >> 1) * 256 + sp * 32 + (u & 1) * 2) * 512;
  u32 wso = (half * 224 + wave * 56) * ld + cblk * 128, xso = wave * (D * 2);
  u32 wnum = on ? D * ld : 0;
  u32 wvo = (lane >> 3) * ld + (lane & 7) * 16, xvo = lane < 56 ? lane * 16 : OOB;
  u32 kvo = (n >> 2) * 4096 + g * 64 + (n & 3) * 16;
  asm volatile("" : "+s"(ld), "+s"(slab), "+s"(tile), "+s"(wso), "+s"(xso), "+s"(wnum));
  asm volatile("" : "+v"(wvo), "+v"(xvo), "+v"(kvo));
  SCHED();
  asm volatile("s_waitcnt lgkmcnt(0)" : "+s"(ka), "+s"(kb));
  SCHED();
  const Args a = {(const u16*)ka[0], (const u16*)ka[1], (const u16*)ka[2], (const u16*)ka[3], (const u16*)ka[4],
                  (const u16*)ka[5], (const u16*)ka[6], (const u16*)ka[7], (u16*)kb[0], (char*)kb[1]};
  const rsrc_t wsr = mkrsrc(a.ws, WS_END);
  const rsrc_t kr = mkrsrc((const char*)a.kc + slab, W * DH * 2), vr = mkrsrc((const char*)a.vc + slab, W * DH * 2);
  const u32 stage_off = (lane >> 3) * 160 + (lane & 7) * 16;
  const u32 tr_off = g * BLK + (n >> 2) * 160 + (n & 3) * 8 + u * 32;
  u32 tag = 0;
  int cap = SPIN_CAP;
  u64 more;

  u32x4 xv, w1[7];
  u32 ep;
  if (svc) {
    xv = ld128<0>(mkrsrc(a.x, B * D * 2), xvo, xso);
    ep = __builtin_amdgcn_raw_buffer_load_b32(wsr, 0, WS_EPOCH, SC1);
    const rsrc_t wr = mkrsrc(isq ? a.wq : (mat ? a.wup : a.wgate), wnum);
#pragma unroll
    for (int i = 0; i < 7; ++i) w1[i] = ld128<NT>(wr, wvo, wso + i * 8 * ld);
  }
  u32x4 kf[8], vf[8];
#pragma unroll
  for (int i = 0; i < 8; ++i) { kf[i] = ld128<NT>(kr, kvo + kv_off(i), tile); SCHED(); }

  if (svc) {
    asm volatile("s_waitcnt vmcnt(12)" ::: "memory");
    wg_barrier();
    __builtin_amdgcn_s_setprio(3);
    float v[8];
#pragma unroll
    for (int i = 0; i < 4; ++i) { v[2 * i] = bflo(xv[i]); v[2 * i + 1] = bfhi(xv[i]); }
    const float mean = wave_sum(((v[0] + v[1]) + (v[2] + v[3])) + ((v[4] + v[5]) + (v[6] + v[7]))) * (1.f / D);
    float q2 = 0.f;
#pragma unroll
    for (int i = 0; i < 8; ++i) q2 += (v[i] - mean) * (v[i] - mean);
    const float var = wave_sum(lane < 56 ? q2 : 0.f) * (1.f / D);
    const float inv = __builtin_amdgcn_rcpf(rbf(__builtin_amdgcn_sqrtf(rbf(rbf(var) + 1e-5f)))), mb = rbf(mean);
    u32x4 xn;
#pragma unroll
    for (int i = 0; i < 4; ++i) xn[i] = pack2(rbf(v[2 * i] - mb) * inv, rbf(v[2 * i + 1] - mb) * inv);
    if (lane < 56) *(LAS u32x4*)(lds + L_XN + wave * 960 + lane * 16) = xn;
    tag = (u32)__builtin_amdgcn_readfirstlane(ep) + 0x9E3779B9u;
    if (tid == 0) *(LAS u32*)(lds + L_CNT + C_TAG) = tag;
#pragma unroll
    for (int i = 0; i < 7; ++i) *(LAS u32x4*)(lds + L_W1 + (wave * 7 + i) * BLK + stage_off) = w1[i];
    lds_signal<C_STAGE>(cbase);
    lds_wait<C_STAGE, 4, 1>(cbase);
    f32x4 acc = {0.f, 0.f, 0.f, 0.f};
#pragma unroll
    for (int ks = 0; ks < 7; ++ks) {
      const u32x4 xa = *(LAS u32x4*)(lds + L_XN + (n & 3) * 960 + half * 448 + ks * 64 + g * 16);
      acc = mfma16(tr_frag(lds + L_W1 + ks * 4 * BLK + tr_off), xa, acc);
    }
    const u32 pcol = isq ? cblk * 64 : HQ * DH + mat * F + cblk * 64;
    const float v3 = rn24(acc[3]);
    const u32 w0 = rec_word(rn24(acc[0]), v3, 0), w1_ = rec_word(rn24(acc[1]), v3, 1), w2 = rec_word(rn24(acc[2]), v3, 2);
    if (on && n < 4)
      __builtin_amdgcn_raw_buffer_store_b128(u32x4{w0, w1_, w2, rec_check(w0, w1_, w2, tag)}, wsr, n * P1_ROW + g * 16,
                                             WS_P1 + half * B * P1_ROW + (pcol + wave * 16) * 4, SC1);
  }
  SCHED();
  const u32 qso = WS_P1 + b * P1_ROW + (kvh * 512 + u * 128) * 4, qvo = (lane >> 5) * (B * P1_ROW) + (lane & 31) * 16;
  const u32 hso = ffn_on ? WS_P1 + u * P1_ROW + (HQ * DH + RB * 64) * 4 : qso;
  const u32 hvo = (lane >> 5) * (B * P1_ROW) + (g & 1) * (ffn_on ? F * 4 : 256) + n * 16;
  const u32 vvo = g * 4096 + n * 16;
  u32x4 q0, h0;
  __builtin_amdgcn_s_setprio(1);
  if (svc) {
    q0 = ld128<SC1>(wsr, qvo, qso);
    h0 = ld128<SC1>(wsr, hvo, hso);
  }
  SCHED();
#pragma unroll
  for (int i = 0; i < 8; ++i) { vf[i] = ld128<NT>(vr, vvo + kv_off(i), tile); SCHED(); }
  __builtin_amdgcn_s_setprio(3);

  if (svc) {
    if (!none(rec_bad(q0, tag) | rec_bad(h0, tag))) {
#pragma nounroll
      POLL_BEGIN();
        q0 = ld128<SC1>(wsr, qvo, qso);
        h0 = ld128<SC1>(wsr, hvo, hso);
        POLL_END(rec_bad(q0, tag) | rec_bad(h0, tag));
    }
    *(LAS u32*)(lds + L_QS + (wave >> 1) * 576 + (wave & 1) * 256 + (lane & 31) * 8 + (lane >> 5) * 4) =
        pack2(fold32(rec_v(q0[0]), rec_v(q0[2])), fold32(rec_v(q0[1]), rec_v3(q0)));
    const u32 gu = pack2(fold32(rec_v(h0[0]), rec_v(h0[2])), fold32(rec_v(h0[1]), rec_v3(h0)));
    const auto sw = __builtin_amdgcn_permlane16_swap(gu, gu, false, false);
    const float g0 = bflo(sw[0]), g1 = bfhi(sw[0]);
    const u32 hh = pack2((g0 > 0.f ? g0 : 0.f) * bflo(sw[1]), (g1 > 0.f ? g1 : 0.f) * bfhi(sw[1]));
    if (!(g & 1)) *(LAS u32*)(lds + L_HS + wave * 192 + n * 8 + (lane >> 5) * 4) = ffn_on ? hh : 0u;
    lds_signal<C_Q>(cbase);
  }
  lds_wait<C_Q, 4, 4>(cbase);

  const u32x4 qa = *(LAS u32x4*)(lds + L_QS + (n >> 3) * 576 + g * 64 + (n & 3) * 16);
  const u32x4 qb = *(LAS u32x4*)(lds + L_QS + (n >> 3) * 576 + g * 64 + (n & 3) * 16 + 256);
  tag = __builtin_amdgcn_readfirstlane(*(LAS u32*)(lds + L_CNT + C_TAG));
  float sc[4];
#pragma unroll
  for (int i = 0; i < 4; ++i) {
    const f32x4 t = mfma16(kf[2 * i + 1], qb, mfma16(kf[2 * i], qa, f32x4{0.f, 0.f, 0.f, 0.f}));
    float s = dpp<0x00>(t[0]);
    s += dpp<0x55>(t[1]); PIN(s);
    s += dpp<0xAA>(t[2]); PIN(s);
    s += dpp<0xFF>(t[3]);
    sc[i] = s;
  }
  {
    const u32 s01 = pack2(sc[0], sc[1]), s23 = pack2(sc[2], sc[3]);
    sc[0] = bflo(s01); sc[1] = bfhi(s01); sc[2] = bflo(s23); sc[3] = bfhi(s23);
  }
  constexpr float C2 = 0.0625f * LOG2E;
  const float m = rows_max(vmax(vmax(sc[0], sc[1]), vmax(sc[2], sc[3]))) * C2;
  u32 pp[4];
#pragma unroll
  for (int i = 0; i < 4; ++i) { const float p = __builtin_amdgcn_exp2f(sc[i] * C2 - m); pp[i] = pack2(p, p); PIN(pp[i]); }
  const float l = rows_sum((bfhi(pp[0]) + bfhi(pp[1])) + (bfhi(pp[2]) + bfhi(pp[3])));
  if ((lane & 55) == 0) *(LAS f32x2*)(lds + L_ML + (lane >> 3) * 128 + wave * 8) = f32x2{m, l};
  lds_signal<C_ML>(cbase);
  u32x4 bf[4];
  {
    const u32 hm = (n & 1) ? 0xffff0000u : 0x0000ffffu, q = (n >> 1) & 3;
    const u32x4 msk = {q == 0 ? hm : 0u, q == 1 ? hm : 0u, q == 2 ? hm : 0u, q == 3 ? hm : 0u};
#pragma unroll
    for (int i = 0; i < 4; ++i) { bf[i] = msk & pp[i]; PIN(bf[i]); }
  }
  u32 so_off = wave * 2048 + (n >> 3) * 1024 + (g * 8 + (n & 7)) * 16;
  PIN(so_off);
  SCHED();
  f32x4 o0 = {0.f, 0.f, 0.f, 0.f}, o1 = {0.f, 0.f, 0.f, 0.f};
#pragma unroll
  for (int i = 0; i < 4; ++i) {
    o0 = mfma16(vf[2 * i], bf[i], o0);
    o1 = mfma16(vf[2 * i + 1], bf[i], o1);
  }
  *(LAS f32x4*)(lds + L_SO + so_off) = o0;
  *(LAS f32x4*)(lds + L_SO + so_off + 512) = o1;
  {
    const u32 one = 1;
    SCHED();
    asm volatile("s_cmp_lt_u32 %2, 12\n\ts_waitcnt lgkmcnt(0)\n\ts_cbranch_scc1 1f\n\ts_barrier\n\ts_branch 2f\n1:\n\t"
                 "s_mov_b64 exec, 1\n\tds_add_u32 %0, %1 offset:%3\n\ts_mov_b64 exec, -1\n2:" :: "v"(cbase), "v"(one), "s"(wave), "i"(C_OLD) : "memory", "scc");
    SCHED();
  }

  if (wave >= 8) {
    u32 so = WS_OB + RB * OB_RB + (wave - 8) * 1536;
    u32 og_off = L_OG + ((wave - 8) * 4 + (lane >> 5)) * 272 + (lane & 31) * 8, ovo = lane * 12;
    asm volatile("" : "+v"(og_off), "+v"(ovo));
    asm volatile("" : "+s"(so));
    if (wave < 12) wg_barrier();
    if (tail_on) {
      __builtin_amdgcn_s_sleep(12);
#pragma nounroll
      POLL_BEGIN();
        asm volatile("s_waitcnt lgkmcnt(0)" ::: "memory");
        const u32x3 t0 = __builtin_amdgcn_raw_buffer_load_b96(wsr, ovo, so, SC1);
        SCHED();
        const u32x3 t1 = __builtin_amdgcn_raw_buffer_load_b96(wsr, ovo + 768, so, SC1);
        SCHED();
        *(LAS u32x2*)(lds + og_off) = u32x2{t0[0], t0[1]};
        u32 b0 = (t0[0] + t0[1] + tag) ^ t0[2];
        asm volatile("" : "+v"(b0) :: "memory");
        *(LAS u32x2*)(lds + og_off + 2 * 272) = u32x2{t1[0], t1[1]};
        SCHED();
        POLL_END(b0 | ((t1[0] + t1[1] + tag) ^ t1[2]));
      wg_barrier();
    }
  } else if (wave >= 4) {
    const bool fin = wave == 4 && tail_on;
    u32 oi = ((RB >> 3) * D + cb * 64 + (RB & 6) * 8 + (RB & 1) * 4 + (lane & 3) + (g & 1) * 8) * 2;
    if (!fin || (lane & 44)) oi = OOB;
    const u32 xraw = __builtin_amdgcn_raw_buffer_load_b16(mkrsrc(a.x, B * D * 2), oi, 0, 0);
    if (u & 1) {
      wg_barrier();
    } else {
      lds_wait<C_ML, 16, 1>(cbase);
      const f32x2 ml = *(LAS f32x2*)(lds + L_ML + (u >> 1) * 128 + n * 8);
      const float M = row16_max(ml[0]);
      const float e = __builtin_amdgcn_exp2f(ml[0] - M);
      const float L = row16_sum(e * ml[1]);
      const u32 lse = asu(M + __builtin_amdgcn_logf(L));
      __builtin_amdgcn_raw_buffer_store_b128(u32x4{lse, lse + tag, lse, lse + tag}, wsr, lane < 8 ? lane * 16 : OOB, WS_LSE + ((kvh * 2 + (u >> 1)) * 32 + r) * 128, SC1);
      const float c = e * __builtin_amdgcn_rcpf(L);
      lds_wait<C_OLD, 12, 1>(cbase);
      LAS char* sp_ = lds + L_SO + u * 512 + lane * 16;
      f32x4 ov[12];
#pragma unroll
      for (int i = 0; i < 12; ++i) ov[i] = *(LAS f32x4*)(sp_ + i * 2048);
      SCHED();
      f32x4 acc = {0.f, 0.f, 0.f, 0.f};
#define MRG(i) acc += dpp<BCAST + i>(c) * ov[i];
      MRG(0) MRG(1) MRG(2) MRG(3) MRG(4) MRG(5) MRG(6) MRG(7) MRG(8) MRG(9) MRG(10) MRG(11)
#undef MRG
      float c12 = dpp<BCAST + 12>(c), c13 = dpp<BCAST + 13>(c), c14 = dpp<BCAST + 14>(c), c15 = dpp<BCAST + 15>(c);
      u32 ob_so = WS_OB + (kvh * 4 + u) * OB_RB + r * 384, ob_vo = (lane >> 5) * OB_RB + (lane & 31) * 12;
      asm volatile("" : "+v"(acc), "+v"(c12), "+v"(c13), "+v"(c14), "+v"(c15), "+v"(ob_vo));
      asm volatile("" : "+s"(ob_so));
      wg_barrier();
      const f32x4 y0 = *(LAS f32x4*)(sp_ + 12 * 2048), y1 = *(LAS f32x4*)(sp_ + 13 * 2048);
      const f32x4 y2 = *(LAS f32x4*)(sp_ + 14 * 2048), y3 = *(LAS f32x4*)(sp_ + 15 * 2048);
      SCHED();
      acc += c12 * y0; acc += c13 * y1; acc += c14 * y2; acc += c15 * y3;
      const u32 p0 = pack2(acc[0], acc[1]), p1 = pack2(acc[2], acc[3]), chk = p0 + p1 + tag;
      __builtin_amdgcn_raw_buffer_store_b96(u32x3{p0, p1, chk}, wsr, ob_vo, ob_so, SC1);
    }
    SCHED();
    if (tail_on) {
      if (wave == 5) {
        __builtin_amdgcn_s_sleep(8);
        u32x2 lz;
#pragma nounroll
        POLL_BEGIN();
          lz = ld64<SC1>(wsr, (lane & 31) * 128, WS_LSE + (RB >> 1) * 32 * 128);
          POLL_END((lz[0] + tag) ^ lz[1]);
        const float z = asf(lz[0]);
        const float ez = __builtin_amdgcn_exp2f(z - row8_max(z));
        *(LAS float*)(lds + L_WT + (lane & 31) * 4) = ez * __builtin_amdgcn_rcpf(row8_sum(ez));
      }
      u32 box = WS_SB + (cb * 16 + (RB >> 1)) * BOX + (RB & 1) * 64, bvo = (lane >> 2) * 128 + (lane & 3) * 16;
      asm volatile("" : "+v"(bvo));
      asm volatile("" : "+s"(box));
      wg_barrier();
      if (fin) {
        float xres = bflo(xraw);
        PIN(xres);
        __builtin_amdgcn_s_sleep(18);
        u32x4 t0, t1;
        float e0;
#pragma nounroll
        POLL_BEGIN();
          t0 = ld128<SC1>(wsr, bvo, box);
          SCHED();
          t1 = ld128<SC1>(wsr, bvo + 2048, box);
          u32 b0 = rec_bad(t0, tag);
          e0 = rec_v3(t0);
          asm volatile("" : "+v"(b0), "+v"(e0));
          SCHED();
          POLL_END(b0 | rec_bad(t1, tag));
        const float s0 = asf(t0[0]) + asf(t1[0]), s1 = asf(t0[1]) + asf(t1[1]), d0 = asf(t0[2]) + asf(t1[2]), d1 = e0 + rec_v3(t1);
        float y = fold32(fold16(s0, s1), fold16(d0, d1));
        y += dpp<XOR8>(y);
        y += dpp<ROR4>(y);
        y = rbf(y);
        const auto sd = __builtin_amdgcn_permlane32_swap(asu(y), asu(y), false, false);
        const float dsum = asf(sd[1]);
        const float gate = rbf(dsum * __builtin_amdgcn_rcpf(1.f + __builtin_amdgcn_exp2f(dsum * -LOG2E)));
        __builtin_amdgcn_raw_buffer_store_b16((u16)pack2(xres + rbf(asf(sd[0]) * gate), 0.f), mkrsrc(a.out, B * D * 2), oi, 0, 0);
      }
    } else if (wg == 255 && wave == 5) {
#pragma nounroll
      POLL_BEGIN();
        __builtin_amdgcn_s_sleep(16);
        u32 x = 0;
#pragma unroll
        for (int i = 0; i < 4; ++i) {
          const u32x2 t = ld64<SC1>(wsr, (lane & 31) * 128 + (lane >> 5) * 8192, WS_LSE + i * 16384);
          x |= (t[0] + tag) ^ t[1];
        }
        POLL_END(x);
      if (lane == 0) __builtin_amdgcn_raw_buffer_store_b32(tag, wsr, 0, WS_EPOCH, SC1);
    }
  } else if (tail_on) {
    const rsrc_t wor = mkrsrc(a.wo, HQ * DH * D * 2), wdr = mkrsrc(a.wdown, ffn_on ? F * D * 2 : 0);
    u32 d_vo = (lane >> 3) * (D * 2) + (lane & 7) * 16, o_vo = (8 * ((lane >> 3) & 3) + (lane >> 5)) * (D * 2) + (lane & 7) * 16;
    u32 d_so = (RB * 64 + 16 * wave) * (D * 2) + cb * 128, o_so = (RB * 128 + 32 * wave) * (D * 2) + cb * 128;
    asm volatile("" : "+v"(d_vo), "+v"(o_vo));
    asm volatile("" : "+s"(d_so), "+s"(o_so));
    const int rb2 = wave >> 1, ch = wave & 1;
    u32 st_d = L_WD + wave * 2 * BLK + stage_off, st_o = L_WL + wave * 4 * BLK + stage_off;
    u32 tr_d = L_WD + g * BLK + (n >> 2) * 160 + (n & 3) * 8 + ch * 64, tr_o = tr_d + (L_WL - L_WD);
    u32 og_off = L_OG + (rb2 * 16 + n) * 272 + g * 16, wt_off = L_WT + rb2 * 64 + g * 16;
    u32 sb_vo = (n & 8) ? OOB : ((g >> 1) * 4 + (g & 1)) * BOX + n * 16, sb_so = WS_SB + (cb * 16 + rb2 * 8 + ch * 2) * BOX + RB * 128;
    u32x4 ha0 = *(LAS u32x4*)(lds + L_HS + (rb2 * 2 + (n >> 3)) * 192 + g * 16);
    u32x4 ha1 = *(LAS u32x4*)(lds + L_HS + (rb2 * 2 + (n >> 3)) * 192 + 64 + g * 16);
    asm volatile("" : "+v"(st_d), "+v"(st_o), "+v"(tr_d), "+v"(tr_o), "+v"(og_off), "+v"(wt_off), "+v"(sb_vo), "+v"(ha0), "+v"(ha1));
    asm volatile("" : "+s"(sb_so));
    SCHED();
    __builtin_amdgcn_s_setprio(0);
    lds_wait<C_OLD, 12, 2>(cbase);
    u32x4 wl[6];
#pragma unroll
    for (int i = 0; i < 2; ++i) wl[i] = ld128<NT>(wdr, d_vo, d_so + 8 * i * (D * 2));
#pragma unroll
    for (int i = 0; i < 4; ++i) wl[2 + i] = ld128<NT>(wor, o_vo + (i & 1) * 2 * (D * 2), o_so + (i & 2) * 2 * (D * 2));
    SCHED();
    __builtin_amdgcn_s_setprio(2);
    wg_barrier();
#pragma unroll
    for (int i = 0; i < 2; ++i) *(LAS u32x4*)(lds + st_d + i * BLK) = wl[i];
#pragma unroll
    for (int i = 0; i < 4; ++i) *(LAS u32x4*)(lds + st_o + i * BLK) = wl[2 + i];
    lds_signal<C_WO>(cbase);
    lds_wait<C_WO, 4, 0>(cbase);
    u32x4 bd[4], bw[8];
#pragma unroll
    for (int i = 0; i < 4; ++i) bd[i] = tr_frag(lds + tr_d + (i >> 1) * 4 * BLK + (i & 1) * 32);
#pragma unroll
    for (int i = 0; i < 8; ++i) bw[i] = tr_frag(lds + tr_o + (i >> 1) * 4 * BLK + (i & 1) * 32);
    SCHED();
    const f32x4 z4 = {0.f, 0.f, 0.f, 0.f};
    const f32x4 da0 = mfma16(ha1, bd[2], mfma16(ha0, bd[0], z4)), da1 = mfma16(ha1, bd[3], mfma16(ha0, bd[1], z4));
    const float dn = (g & 1) ? da1[0] : da0[0];
    float dn1 = dpp<XOR8>(dn);
    u32 w2 = rec_word(dn, dn1, 2), w2t = w2 + tag;
    asm volatile("" : "+v"(dn1), "+v"(w2), "+v"(w2t), "+v"(bw[0]), "+v"(bw[1]), "+v"(bw[2]), "+v"(bw[3]), "+v"(bw[4]), "+v"(bw[5]), "+v"(bw[6]), "+v"(bw[7]));
    wg_barrier();
    u32x4 oa[4];
#pragma unroll
    for (int ks = 0; ks < 4; ++ks) oa[ks] = *(LAS u32x4*)(lds + og_off + ks * 64);
    const f32x4 wt = *(LAS f32x4*)(lds + wt_off);
    SCHED();
    f32x4 a0 = z4, a1 = z4;
#pragma unroll
    for (int ks = 0; ks < 4; ++ks) {
      a0 = mfma16(oa[ks], bw[2 * ks], a0);
      a1 = mfma16(oa[ks], bw[2 * ks + 1], a1);
    }
    float s0 = a0[0] * wt[0];
    s0 = fmaf(a0[1], wt[1], s0); s0 = fmaf(a0[2], wt[2], s0); s0 = fmaf(a0[3], wt[3], s0);
    PIN(s0);
    float s1 = a1[0] * wt[0];
    s1 = fmaf(a1[1], wt[1], s1); s1 = fmaf(a1[2], wt[2], s1); s1 = fmaf(a1[3], wt[3], s1);
    const float sv = fold16(s0, s1);
    const u32 r0 = rec_word(sv, dn1, 0), r1 = rec_word(dpp<XOR8>(sv), dn1, 1);
    __builtin_amdgcn_raw_buffer_store_b128(u32x4{r0, r1, w2, r0 + r1 + w2t}, wsr, sb_vo, sb_so, SC1);
  }
}

extern "C" void kernel_launch(void* const* d_in, const int* in_sizes, int n_in, void* d_out, int out_size, void* d_ws, size_t ws_size,
                              hipStream_t stream) {
  Args a;
  a.kc = (const u16*)d_in[1];
  a.vc = (const u16*)d_in[2];
  a.x = (const u16*)d_in[0];
  a.wq = (const u16*)d_in[3];
  a.wgate = (const u16*)d_in[6];
  a.wup = (const u16*)d_in[7];
  a.wdown = (const u16*)d_in[8];
  a.wo = (const u16*)d_in[4];
  a.out = (u16*)d_out;
  a.ws = (char*)d_ws;
  fused_decode<<<256, 1024, 0, stream>>>(a);
}
```

```cpp
#include <hip/hip_runtime.h>
#include <stdint.h>

typedef unsigned u32;
typedef unsigned short u16;
typedef unsigned long long u64;
typedef u32 u32x2 __attribute__((ext_vector_type(2)));
typedef u32 u32x3 __attribute__((ext_vector_type(3)));
typedef u32 u32x4 __attribute__((ext_vector_type(4)));
typedef u64 u64x2 __attribute__((ext_vector_type(2)));
typedef u64 u64x8 __attribute__((ext_vector_type(8)));
typedef float f32x2 __attribute__((ext_vector_type(2)));
typedef float f32x4 __attribute__((ext_vector_type(4)));
typedef __bf16 bf16x2 __attribute__((ext_vector_type(2)));
typedef __bf16 bf16x8 __attribute__((ext_vector_type(8)));
typedef short s16x4 __attribute__((ext_vector_type(4)));
typedef __amdgpu_buffer_rsrc_t rsrc_t;
#define LAS __attribute__((address_space(3)))

constexpr int B = 4, D = 448, HQ = 16, HKV = 8, SK = 8192, DH = 256, W = 2048, F = 1792;
constexpr int NT = 2, SC1 = 16;
constexpr u32 OOB = 0x40000000u;
constexpr u32 SPIN_CAP = 1u << 17;
constexpr float LOG2E = 1.4426950408889634f;

constexpr u32 P1_ROW = (HQ * DH + 2 * F + 64) * 4;
constexpr u32 OB_RB = 33 * 384;
constexpr u32 BOX = 32 * 128 + 128;
constexpr u32 WS_EPOCH = 0;
constexpr u32 WS_ML = 4096;
constexpr u32 WS_P1 = WS_ML + HQ * 32 * 128;
constexpr u32 WS_OB = WS_P1 + 2 * B * P1_ROW;
constexpr u32 WS_SB = WS_OB + HKV * 4 * OB_RB;
constexpr u32 WS_END = WS_SB + 112 * BOX;

constexpr int BLK = 1408;
constexpr int L_CNT = 0;
constexpr int L_ML = 64;
constexpr int L_WT = L_ML + 256;
constexpr int L_QS = L_WT + 128;
constexpr int L_HS = L_QS + 2 * 576;
constexpr int L_XN = L_HS + 4 * 192;
constexpr int L_XR = L_XN + 4 * 960;
constexpr int L_OG = L_XR + B * D * 2;
constexpr int L_SO = L_OG + 32 * 272;
constexpr int L_WD = L_SO + 16 * 2048;
constexpr int L_W1 = L_WD + 8 * BLK;
constexpr int L_WL = L_W1 + 28 * BLK;
constexpr int L_END = L_WL + 16 * BLK;
enum { C_STAGE = 0, C_Q = 4, C_ML = 8, C_OLD = 12, C_WO = 16, C_TAG = 28 };

struct Args {
  const u16 *kc, *vc, *x, *wq, *wgate, *wup, *wdown, *wo;
  u16* out;
  char* ws;
};

__device__ __forceinline__ float asf(u32 x) { return __uint_as_float(x); }
__device__ __forceinline__ u32 asu(float x) { return __float_as_uint(x); }
__device__ __forceinline__ float bflo(u32 x) { return asf(x << 16); }
__device__ __forceinline__ float bfhi(u32 x) { return asf(x & 0xffff0000u); }
__device__ __forceinline__ float rbf(float x) { return (float)(__bf16)x; }
__device__ __forceinline__ u32 pack2(float lo, float hi) { bf16x2 v = {(__bf16)lo, (__bf16)hi}; return __builtin_bit_cast(u32, v); }
__device__ __forceinline__ rsrc_t mkrsrc(const void* p, u32 bytes) { return __builtin_amdgcn_make_buffer_rsrc((void*)p, 0, bytes, 0x00020000); }
template <int AUX> __device__ __forceinline__ u32x4 ld128(rsrc_t r, u32 voff, u32 soff) { return __builtin_amdgcn_raw_buffer_load_b128(r, voff, soff, AUX); }
__device__ __forceinline__ bool none(u32 x) { return __builtin_amdgcn_ballot_w64(x != 0) == 0; }

__device__ __forceinline__ float rn24(float v) { return asf(asu(v) + 0x80u); }
__device__ __forceinline__ u32 rec_word(float v, float v3, int i) { return __builtin_amdgcn_perm(asu(v), asu(v3), 0x07060503u - i); }
__device__ __forceinline__ u32 rec_check(u32 w0, u32 w1, u32 w2, u32 tag) { return w0 + w1 + w2 + tag; }
__device__ __forceinline__ u32 rec_bad(u32x4 v, u32 tag) { return (v[0] + v[1] + tag) ^ (v[3] - v[2]); }
__device__ __forceinline__ u32 ml_bad(u32x4 v, u32 tag) { return ((v[0] + tag + v[1]) ^ v[2]) | ((v[0] + tag - v[1]) ^ v[3]); }
__device__ __forceinline__ float rec_v(u32 w) { return asf(w & 0xffffff00u); }
__device__ __forceinline__ float rec_v3(u32x4 v) { return asf(__builtin_amdgcn_perm(__builtin_amdgcn_perm(v[0], v[1], 0x0400000cu), v[2], 0x0706000cu)); }

__device__ __forceinline__ f32x4 mfma16(u32x4 a, u32x4 b, f32x4 c) {
  return __builtin_amdgcn_mfma_f32_16x16x32_bf16(__builtin_bit_cast(bf16x8, a), __builtin_bit_cast(bf16x8, b), c, 0, 0, 0);
}
template <int CTRL> __device__ __forceinline__ float dpp(float x) {
  return __builtin_bit_cast(float, __builtin_amdgcn_mov_dpp(__builtin_bit_cast(int, x), CTRL, 0xf, 0xf, true));
}
constexpr int XOR1 = 0xB1, XOR2 = 0x4E, XOR7 = 0x141, XOR8 = 0x128, ROR4 = 0x124, BCAST = 0x150;
__device__ __forceinline__ float vmax(float a, float b) { float r; asm("v_max_f32 %0, %1, %2" : "=v"(r) : "v"(a), "v"(b)); return r; }
__device__ __forceinline__ float fold32(float lo, float hi) { const auto s = __builtin_amdgcn_permlane32_swap(asu(lo), asu(hi), false, false); return asf(s[0]) + asf(s[1]); }
__device__ __forceinline__ float fold16(float ev, float od) { const auto s = __builtin_amdgcn_permlane16_swap(asu(ev), asu(od), false, false); return asf(s[0]) + asf(s[1]); }
__device__ __forceinline__ float rows_sum(float x) { x = fold16(x, x); return fold32(x, x); }
__device__ __forceinline__ float rows_max(float x) {
  const auto s = __builtin_amdgcn_permlane16_swap(asu(x), asu(x), false, false);
  x = vmax(asf(s[0]), asf(s[1]));
  const auto t = __builtin_amdgcn_permlane32_swap(asu(x), asu(x), false, false);
  return vmax(asf(t[0]), asf(t[1]));
}
__device__ __forceinline__ float row8_sum(float x) { x += dpp<XOR1>(x); x += dpp<XOR2>(x); return x + dpp<XOR7>(x); }
__device__ __forceinline__ float row16_sum(float x) { x = row8_sum(x); return x + dpp<XOR8>(x); }
__device__ __forceinline__ float row8_max(float x) {
  asm("s_nop 1\n\tv_max_f32_dpp %0, %0, %0 quad_perm:[1,0,3,2] row_mask:0xf bank_mask:0xf\n\t"
      "s_nop 1\n\tv_max_f32_dpp %0, %0, %0 quad_perm:[2,3,0,1] row_mask:0xf bank_mask:0xf\n\t"
      "s_nop 1\n\tv_max_f32_dpp %0, %0, %0 row_half_mirror row_mask:0xf bank_mask:0xf\n\ts_nop 1" : "+v"(x));
  return x;
}
__device__ __forceinline__ float row16_max(float x) {
  x = row8_max(x);
  asm("s_nop 1\n\tv_max_f32_dpp %0, %0, %0 row_ror:8 row_mask:0xf bank_mask:0xf\n\ts_nop 1" : "+v"(x));
  return x;
}
__device__ __forceinline__ float wave_sum(float x) { return rows_sum(row16_sum(x)); }

#define PIN(x) asm volatile("" : "+v"(x))
#define SCHED() __builtin_amdgcn_sched_barrier(0)
#define POLL_BEGIN() do { u32 lim = (u32)(--cap >> 31); asm volatile("" : "+s"(lim) :: "memory")
#define POLL_END(mismatch) more = __builtin_amdgcn_ballot_w64((mismatch) > lim); } while (more)
__device__ __forceinline__ void wg_barrier() { SCHED(); asm volatile("s_waitcnt lgkmcnt(0)\n\ts_barrier" ::: "memory"); SCHED(); }
template <int OFF> __device__ __forceinline__ void lds_signal(u32 base) {
  const u32 one = 1;
  asm volatile("s_waitcnt lgkmcnt(0)\n\ts_mov_b64 exec, 1\n\tds_add_u32 %0, %1 offset:%2\n\ts_mov_b64 exec, -1" :: "v"(base), "v"(one), "i"(OFF) : "memory");
}
template <int OFF, int WANT, int SLEEP> __device__ __forceinline__ void lds_wait(u32 base) {
  u32 v, s, cap = SPIN_CAP * 8;
  asm volatile("1:\n\tds_read_b32 %0, %3 offset:%4\n\ts_waitcnt lgkmcnt(0)\n\tv_readfirstlane_b32 %1, %0\n\ts_cmp_ge_u32 %1, %5\n\ts_cbranch_scc1 2f\n\t"
               "s_sleep %6\n\ts_sub_u32 %2, %2, 1\n\ts_cbranch_scc0 1b\n2:"
               : "=&v"(v), "=&s"(s), "+s"(cap) : "v"(base), "i"(OFF), "i"(WANT), "i"(SLEEP) : "memory", "scc");
}
__device__ __forceinline__ u32x4 tr_frag(LAS char* p) {
  const s16x4 a = __builtin_amdgcn_ds_read_tr16_b64_v4i16((LAS s16x4*)p);
  const s16x4 b = __builtin_amdgcn_ds_read_tr16_b64_v4i16((LAS s16x4*)(p + 4 * 160));
  const u32x2 x = __builtin_bit_cast(u32x2, a), y = __builtin_bit_cast(u32x2, b);
  return u32x4{x[0], x[1], y[0], y[1]};
}
constexpr u32 kv_off(int i) { return (i & 1) * 256 + (i & 2) * 1024 + (i & 4) * 128; }

__global__ void __attribute__((aligned(4096))) __launch_bounds__(1024, 4) fused_decode(const Args) {
  u64x8 ka;
  u64x2 kb;
  asm volatile("s_load_dwordx16 %0, %2, 0x0\n\ts_load_dwordx4 %1, %2, 0x40" : "=&s"(ka), "=&s"(kb) : "s"(__builtin_amdgcn_kernarg_segment_ptr()));
  SCHED();
  __shared__ __attribute__((aligned(16))) char smem[L_END];
  LAS char* lds = (LAS char*)smem;
  const u32 cbase = (u32)(uintptr_t)(lds + L_CNT);
  __builtin_amdgcn_s_setprio(2);
  const int wg = blockIdx.x, tid = threadIdx.x, lane = tid & 63;
  const int wave = __builtin_amdgcn_readfirstlane(tid >> 6), u = wave & 3;
  {
    const u32 zero = 0, addr = cbase + (tid & 7) * 4;
    asm volatile("s_cmp_lt_u32 %2, 4\n\ts_cbranch_scc1 1f\n\ts_cmp_lt_u32 %2, 15\n\ts_cbranch_scc1 2f\n\tds_write_b32 %0, %1\n\ts_waitcnt lgkmcnt(0)\n2:\n\ts_barrier\n1:"
                 :: "v"(addr), "v"(zero), "s"(wave) : "memory", "scc");
    SCHED();
  }
  const int kvh = wg & 7, r = wg >> 3, b = r >> 3, sp = r & 7;
  const int n = lane & 15, g = lane >> 4;
  const int rbc = (r * 37) >> 8, cb = r - rbc * 7, RB = (kvh ^ 5) * 4 + rbc, RF = ((kvh + 3) & 7) * 4 + rbc;
  const bool tail_on = r < 28, ffn_on = tail_on && RF < 28;
  const int odd = kvh & 1, gq = kvh >> 1, t1 = r >> 1, hi = (r + 4) >> 5, iq = odd | hi;
  const bool svc = wave < 4, isq = iq, on = !(odd & hi);
  const int half = r & 1, mat = t1 & 1;
  const int cq = odd * (t1 + ((t1 + 13) >> 4) + ((t1 + 6) >> 4)) + (1 - odd) * (3 + 8 * mat);
  const int cblk = iq * (gq * 16 + cq) + (1 - iq) * (gq * 7 + (r >> 2));
  u32 ld = F * 2 + iq * (HQ * DH * 2 - F * 2);
  u32 slab = ((b * HKV + kvh) * SK + (SK - W)) * DH * 2;
  u32 tile = ((wave >> 1) * 256 + sp * 32 + (u & 1) * 2) * 512;
  u32 wso = (half * 224 + wave * 56) * ld + cblk * 128, xso = wave * (D * 2);
  u32 wnum = on ? D * ld : 0;
  u32 wvo = (lane >> 3) * ld + (lane & 7) * 16, xvo = lane < 56 ? lane * 16 : OOB;
  u32 kvo = (n >> 2) * 4096 + g * 64 + (n & 3) * 16;
  asm volatile("" : "+s"(ld), "+s"(slab), "+s"(tile), "+s"(wso), "+s"(xso), "+s"(wnum));
  asm volatile("" : "+v"(wvo), "+v"(xvo), "+v"(kvo));
  SCHED();
  asm volatile("s_waitcnt lgkmcnt(0)" : "+s"(ka), "+s"(kb));
  SCHED();
  const Args a = {(const u16*)ka[0], (const u16*)ka[1], (const u16*)ka[2], (const u16*)ka[3], (const u16*)ka[4],
                  (const u16*)ka[5], (const u16*)ka[6], (const u16*)ka[7], (u16*)kb[0], (char*)kb[1]};
  const rsrc_t wsr = mkrsrc(a.ws, WS_END);
  const rsrc_t kr = mkrsrc((const char*)a.kc + slab, W * DH * 2), vr = mkrsrc((const char*)a.vc + slab, W * DH * 2);
  const u32 stage_off = (lane >> 3) * 160 + (lane & 7) * 16;
  const u32 tr_off = g * BLK + (n >> 2) * 160 + (n & 3) * 8 + u * 32;
  u32 tag = 0;
  int cap = SPIN_CAP;
  u64 more;

  u32x4 xv, w1[7];
  u32 ep;
  if (svc) {
    const rsrc_t wr = mkrsrc(isq ? a.wq : (mat ? a.wup : a.wgate), wnum);
#pragma unroll
    for (int i = 0; i < 7; ++i) w1[i] = ld128<NT>(wr, wvo, wso + i * 8 * ld);
    SCHED();
    xv = ld128<0>(mkrsrc(a.x, B * D * 2), xvo, xso);
    ep = __builtin_amdgcn_raw_buffer_load_b32(wsr, 0, WS_EPOCH, SC1);
  }
  u32x4 kf[8], vf[8];
#pragma unroll
  for (int i = 0; i < 8; ++i) { kf[i] = ld128<NT>(kr, kvo + kv_off(i), tile); SCHED(); }

  if (svc) {
    asm volatile("s_waitcnt vmcnt(14)" ::: "memory");
    wg_barrier();
    __builtin_amdgcn_s_setprio(3);
    float v[8];
#pragma unroll
    for (int i = 0; i < 4; ++i) { v[2 * i] = bflo(xv[i]); v[2 * i + 1] = bfhi(xv[i]); }
    const float mean = wave_sum(((v[0] + v[1]) + (v[2] + v[3])) + ((v[4] + v[5]) + (v[6] + v[7]))) * (1.f / D);
    float q2 = 0.f;
#pragma unroll
    for (int i = 0; i < 8; ++i) q2 += (v[i] - mean) * (v[i] - mean);
    const float var = wave_sum(lane < 56 ? q2 : 0.f) * (1.f / D);
    const float inv = __builtin_amdgcn_rcpf(rbf(__builtin_amdgcn_sqrtf(rbf(rbf(var) + 1e-5f)))), mb = rbf(mean);
    u32x4 xn;
#pragma unroll
    for (int i = 0; i < 4; ++i) xn[i] = pack2(rbf(v[2 * i] - mb) * inv, rbf(v[2 * i + 1] - mb) * inv);
    if (lane < 56) {
      *(LAS u32x4*)(lds + L_XN + wave * 960 + lane * 16) = xn;
      *(LAS u32x4*)(lds + L_XR + wave * (D * 2) + lane * 16) = xv;
    }
    tag = (u32)__builtin_amdgcn_readfirstlane(ep) + 0x9E3779B9u;
    if (tid == 0) *(LAS u32*)(lds + L_CNT + C_TAG) = tag;
#pragma unroll
    for (int i = 0; i < 7; ++i) *(LAS u32x4*)(lds + L_W1 + (wave * 7 + i) * BLK + stage_off) = w1[i];
    lds_signal<C_STAGE>(cbase);
    lds_wait<C_STAGE, 4, 1>(cbase);
    f32x4 acc = {0.f, 0.f, 0.f, 0.f};
#pragma unroll
    for (int ks = 0; ks < 7; ++ks) {
      const u32x4 xa = *(LAS u32x4*)(lds + L_XN + (n & 3) * 960 + half * 448 + ks * 64 + g * 16);
      acc = mfma16(tr_frag(lds + L_W1 + ks * 4 * BLK + tr_off), xa, acc);
    }
    const u32 pcol = isq ? cblk * 64 : HQ * DH + mat * F + cblk * 64;
    const float v3 = rn24(acc[3]);
    const u32 w0 = rec_word(rn24(acc[0]), v3, 0), w1_ = rec_word(rn24(acc[1]), v3, 1), w2 = rec_word(rn24(acc[2]), v3, 2);
    if (on && n < 4)
      __builtin_amdgcn_raw_buffer_store_b128(u32x4{w0, w1_, w2, rec_check(w0, w1_, w2, tag)}, wsr, n * P1_ROW + g * 16,
                                             WS_P1 + half * B * P1_ROW + (pcol + wave * 16) * 4, SC1);
  }
  SCHED();
  const u32 qso = WS_P1 + b * P1_ROW + (kvh * 512 + u * 128) * 4, qvo = (lane >> 5) * (B * P1_ROW) + (lane & 31) * 16;
  const u32 hso = ffn_on ? WS_P1 + u * P1_ROW + (HQ * DH + RF * 64) * 4 : qso;
  const u32 hvo = (lane >> 5) * (B * P1_ROW) + (g & 1) * (ffn_on ? F * 4 : 256) + n * 16;
  const u32 vvo = g * 4096 + n * 16;
  u32x4 q0, h0;
  __builtin_amdgcn_s_setprio(1);
  if (svc) {
    q0 = ld128<SC1>(wsr, qvo, qso);
    h0 = ld128<SC1>(wsr, hvo, hso);
  }
  SCHED();
#pragma unroll
  for (int i = 0; i < 8; ++i) { vf[i] = ld128<NT>(vr, vvo + kv_off(i), tile); SCHED(); }
  __builtin_amdgcn_s_setprio(3);

  if (svc) {
    if (!none(rec_bad(q0, tag) | rec_bad(h0, tag))) {
#pragma nounroll
      POLL_BEGIN();
        q0 = ld128<SC1>(wsr, qvo, qso);
        h0 = ld128<SC1>(wsr, hvo, hso);
        POLL_END(rec_bad(q0, tag) | rec_bad(h0, tag));
    }
    *(LAS u32*)(lds + L_QS + (wave >> 1) * 576 + (wave & 1) * 256 + (lane & 31) * 8 + (lane >> 5) * 4) =
        pack2(fold32(rec_v(q0[0]), rec_v(q0[2])), fold32(rec_v(q0[1]), rec_v3(q0)));
    const u32 gu = pack2(fold32(rec_v(h0[0]), rec_v(h0[2])), fold32(rec_v(h0[1]), rec_v3(h0)));
    const auto sw = __builtin_amdgcn_permlane16_swap(gu, gu, false, false);
    const float g0 = bflo(sw[0]), g1 = bfhi(sw[0]);
    const u32 hh = pack2((g0 > 0.f ? g0 : 0.f) * bflo(sw[1]), (g1 > 0.f ? g1 : 0.f) * bfhi(sw[1]));
    if (!(g & 1)) *(LAS u32*)(lds + L_HS + wave * 192 + n * 8 + (lane >> 5) * 4) = ffn_on ? hh : 0u;
    lds_signal<C_Q>(cbase);
  }
  lds_wait<C_Q, 4, 4>(cbase);

  const u32x4 qa = *(LAS u32x4*)(lds + L_QS + (n >> 3) * 576 + g * 64 + (n & 3) * 16);
  const u32x4 qb = *(LAS u32x4*)(lds + L_QS + (n >> 3) * 576 + g * 64 + (n & 3) * 16 + 256);
  tag = __builtin_amdgcn_readfirstlane(*(LAS u32*)(lds + L_CNT + C_TAG));
  float sc[4];
#pragma unroll
  for (int i = 0; i < 4; ++i) {
    const f32x4 t = mfma16(kf[2 * i + 1], qb, mfma16(kf[2 * i], qa, f32x4{0.f, 0.f, 0.f, 0.f}));
    float s = dpp<0x00>(t[0]);
    s += dpp<0x55>(t[1]); PIN(s);
    s += dpp<0xAA>(t[2]); PIN(s);
    s += dpp<0xFF>(t[3]);
    sc[i] = s;
  }
  {
    const u32 s01 = pack2(sc[0], sc[1]), s23 = pack2(sc[2], sc[3]);
    sc[0] = bflo(s01); sc[1] = bfhi(s01); sc[2] = bflo(s23); sc[3] = bfhi(s23);
  }
  constexpr float C2 = 0.0625f * LOG2E;
  const float m = rows_max(vmax(vmax(sc[0], sc[1]), vmax(sc[2], sc[3])));
  u32 pp[4];
#pragma unroll
  for (int i = 0; i < 4; ++i) { const float p = __builtin_amdgcn_exp2f((sc[i] - m) * C2); pp[i] = pack2(p, p); PIN(pp[i]); }
  const float l = rows_sum((bfhi(pp[0]) + bfhi(pp[1])) + (bfhi(pp[2]) + bfhi(pp[3])));
  if ((lane & 55) == 0) *(LAS f32x2*)(lds + L_ML + (lane >> 3) * 128 + wave * 8) = f32x2{m, l};
  lds_signal<C_ML>(cbase);
  u32x4 bf[4];
  {
    const u32 hm = (n & 1) ? 0xffff0000u : 0x0000ffffu, q = (n >> 1) & 3;
    const u32x4 msk = {q == 0 ? hm : 0u, q == 1 ? hm : 0u, q == 2 ? hm : 0u, q == 3 ? hm : 0u};
#pragma unroll
    for (int i = 0; i < 4; ++i) { bf[i] = msk & pp[i]; PIN(bf[i]); }
  }
  u32 so_off = wave * 2048 + (n >> 3) * 1024 + (g * 8 + (n & 7)) * 16;
  PIN(so_off);
  SCHED();
  f32x4 o0 = {0.f, 0.f, 0.f, 0.f}, o1 = {0.f, 0.f, 0.f, 0.f};
#pragma unroll
  for (int i = 0; i < 4; ++i) {
    o0 = mfma16(vf[2 * i], bf[i], o0);
    o1 = mfma16(vf[2 * i + 1], bf[i], o1);
  }
  *(LAS f32x4*)(lds + L_SO + so_off) = o0;
  *(LAS f32x4*)(lds + L_SO + so_off + 512) = o1;
  {
    const u32 one = 1;
    SCHED();
    asm volatile("s_cmp_lt_u32 %2, 12\n\ts_waitcnt lgkmcnt(0)\n\ts_cbranch_scc1 1f\n\ts_barrier\n\ts_branch 2f\n1:\n\t"
                 "s_mov_b64 exec, 1\n\tds_add_u32 %0, %1 offset:%3\n\ts_mov_b64 exec, -1\n2:" :: "v"(cbase), "v"(one), "s"(wave), "i"(C_OLD) : "memory", "scc");
    SCHED();
  }

  if (wave >= 8) {
    u32 so = WS_OB + RB * OB_RB + (wave - 8) * 1536;
    u32 og_off = L_OG + ((wave - 8) * 4 + (lane >> 5)) * 272 + (lane & 31) * 8, ovo = lane * 12;
    asm volatile("" : "+v"(og_off), "+v"(ovo));
    asm volatile("" : "+s"(so));
    if (wave < 12) wg_barrier();
    if (tail_on) {
      __builtin_amdgcn_s_sleep(12);
#pragma nounroll
      POLL_BEGIN();
        asm volatile("s_waitcnt lgkmcnt(0)" ::: "memory");
        const u32x3 t0 = __builtin_amdgcn_raw_buffer_load_b96(wsr, ovo, so, SC1);
        SCHED();
        const u32x3 t1 = __builtin_amdgcn_raw_buffer_load_b96(wsr, ovo + 768, so, SC1);
        SCHED();
        *(LAS u32x2*)(lds + og_off) = u32x2{t0[0], t0[1]};
        u32 b0 = (t0[0] + t0[1] + tag) ^ t0[2];
        asm volatile("" : "+v"(b0) :: "memory");
        *(LAS u32x2*)(lds + og_off + 2 * 272) = u32x2{t1[0], t1[1]};
        SCHED();
        POLL_END(b0 | ((t1[0] + t1[1] + tag) ^ t1[2]));
      wg_barrier();
    }
  } else if (wave >= 4) {
    const bool fin = wave == 4 && tail_on;
    u32 oi = ((RB >> 3) * D + cb * 64 + (RB & 6) * 8 + (RB & 1) * 4 + (lane & 3) + (g & 1) * 8) * 2;
    if (u & 1) {
      wg_barrier();
    } else {
      lds_wait<C_OLD, 12, 1>(cbase);
      LAS char* sp_ = lds + L_SO + u * 512 + lane * 16;
      f32x4 ov[12];
#pragma unroll
      for (int i = 0; i < 12; ++i) ov[i] = *(LAS f32x4*)(sp_ + i * 2048);
      SCHED();
      lds_wait<C_ML, 16, 0>(cbase);
      const f32x2 ml = *(LAS f32x2*)(lds + L_ML + (u >> 1) * 128 + n * 8);
      const float M = row16_max(ml[0]);
      const float e = __builtin_amdgcn_exp2f((ml[0] - M) * C2);
      const float L = row16_sum(e * ml[1]);
      const u32 mt = asu(M) + tag;
      __builtin_amdgcn_raw_buffer_store_b128(u32x4{asu(M), asu(L), mt + asu(L), mt - asu(L)}, wsr, lane < 8 ? lane * 16 : OOB,
                                             WS_ML + ((kvh * 2 + (u >> 1)) * 32 + r) * 128, SC1);
      const float c = e * __builtin_amdgcn_rcpf(L);
      SCHED();
      f32x4 acc = {0.f, 0.f, 0.f, 0.f};
#define MRG(i) acc += dpp<BCAST + i>(c) * ov[i];
      MRG(0) MRG(1) MRG(2) MRG(3) MRG(4) MRG(5) MRG(6) MRG(7) MRG(8) MRG(9) MRG(10) MRG(11)
#undef MRG
      float c12 = dpp<BCAST + 12>(c), c13 = dpp<BCAST + 13>(c), c14 = dpp<BCAST + 14>(c), c15 = dpp<BCAST + 15>(c);
      u32 ob_so = WS_OB + (kvh * 4 + u) * OB_RB + r * 384, ob_vo = (lane >> 5) * OB_RB + (lane & 31) * 12;
      asm volatile("" : "+v"(acc), "+v"(c12), "+v"(c13), "+v"(c14), "+v"(c15), "+v"(ob_vo));
      asm volatile("" : "+s"(ob_so));
      wg_barrier();
      const f32x4 y0 = *(LAS f32x4*)(sp_ + 12 * 2048), y1 = *(LAS f32x4*)(sp_ + 13 * 2048);
      const f32x4 y2 = *(LAS f32x4*)(sp_ + 14 * 2048), y3 = *(LAS f32x4*)(sp_ + 15 * 2048);
      SCHED();
      acc += c12 * y0; acc += c13 * y1; acc += c14 * y2; acc += c15 * y3;
      const u32 p0 = pack2(acc[0], acc[1]), p1 = pack2(acc[2], acc[3]), chk = p0 + p1 + tag;
      __builtin_amdgcn_raw_buffer_store_b96(u32x3{p0, p1, chk}, wsr, ob_vo, ob_so, SC1);
    }
    SCHED();
    if (tail_on) {
      if (wave == 5) {
        __builtin_amdgcn_s_sleep(8);
        u32x4 lz;
#pragma nounroll
        POLL_BEGIN();
          lz = ld128<SC1>(wsr, (lane & 31) * 128, WS_ML + (RB >> 1) * 32 * 128);
          POLL_END(ml_bad(lz, tag));
        const float z = asf(lz[0]);
        const float ez = asf(lz[1]) * __builtin_amdgcn_exp2f((z - row8_max(z)) * C2);
        *(LAS float*)(lds + L_WT + (lane & 31) * 4) = ez * __builtin_amdgcn_rcpf(row8_sum(ez));
      }
      u32 box = WS_SB + (cb * 16 + (RB >> 1)) * BOX + (RB & 1) * 64, bvo = (lane >> 2) * 128 + (lane & 3) * 16;
      asm volatile("" : "+v"(bvo));
      asm volatile("" : "+s"(box));
      wg_barrier();
      if (fin) {
        float xres = bflo(*(LAS u16*)(lds + L_XR + oi));
        if (lane & 44) oi = OOB;
        asm volatile("" : "+v"(xres), "+v"(oi));
        __builtin_amdgcn_s_sleep(18);
        u32x4 t0, t1;
        float e0;
#pragma nounroll
        POLL_BEGIN();
          t0 = ld128<SC1>(wsr, bvo, box);
          SCHED();
          t1 = ld128<SC1>(wsr, bvo + 2048, box);
          u32 b0 = rec_bad(t0, tag);
          e0 = rec_v3(t0);
          asm volatile("" : "+v"(b0), "+v"(e0));
          SCHED();
          POLL_END(b0 | rec_bad(t1, tag));
        const float s0 = asf(t0[0]) + asf(t1[0]), s1 = asf(t0[1]) + asf(t1[1]), d0 = asf(t0[2]) + asf(t1[2]), d1 = e0 + rec_v3(t1);
        float y = fold32(fold16(s0, s1), fold16(d0, d1));
        y += dpp<XOR8>(y);
        y += dpp<ROR4>(y);
        y = rbf(y);
        const auto sd = __builtin_amdgcn_permlane32_swap(asu(y), asu(y), false, false);
        const float dsum = asf(sd[1]);
        const float gate = rbf(dsum * __builtin_amdgcn_rcpf(1.f + __builtin_amdgcn_exp2f(dsum * -LOG2E)));
        __builtin_amdgcn_raw_buffer_store_b16((u16)pack2(xres + rbf(asf(sd[0]) * gate), 0.f), mkrsrc(a.out, B * D * 2), oi, 0, 0);
      }
    } else if (wg == 255 && wave == 5) {
#pragma nounroll
      POLL_BEGIN();
        __builtin_amdgcn_s_sleep(16);
        u32 x = 0;
#pragma unroll
        for (int i = 0; i < 4; ++i) x |= ml_bad(ld128<SC1>(wsr, (lane & 31) * 128 + (lane >> 5) * 8192, WS_ML + i * 16384), tag);
        POLL_END(x);
      if (lane == 0) __builtin_amdgcn_raw_buffer_store_b32(tag, wsr, 0, WS_EPOCH, SC1);
    }
  } else if (tail_on) {
    const rsrc_t wor = mkrsrc(a.wo, HQ * DH * D * 2), wdr = mkrsrc(a.wdown, ffn_on ? F * D * 2 : 0);
    u32 d_vo = (lane >> 3) * (D * 2) + (lane & 7) * 16, o_vo = (8 * ((lane >> 3) & 3) + (lane >> 5)) * (D * 2) + (lane & 7) * 16;
    u32 d_so = (RF * 64 + 16 * wave) * (D * 2) + cb * 128, o_so = (RB * 128 + 32 * wave) * (D * 2) + cb * 128;
    asm volatile("" : "+v"(d_vo), "+v"(o_vo));
    asm volatile("" : "+s"(d_so), "+s"(o_so));
    const int rb2 = wave >> 1, ch = wave & 1;
    u32 st_d = L_WD + wave * 2 * BLK + stage_off, st_o = L_WL + wave * 4 * BLK + stage_off;
    u32 tr_d = L_WD + g * BLK + (n >> 2) * 160 + (n & 3) * 8 + ch * 64, tr_o = tr_d + (L_WL - L_WD);
    u32 og_off = L_OG + (rb2 * 16 + n) * 272 + g * 16, wt_off = L_WT + rb2 * 64 + g * 16;
    u32 sb_vo = (n & 8) ? OOB : ((g >> 1) * 4 + (g & 1)) * BOX + n * 16, sb_so = WS_SB + (cb * 16 + rb2 * 8 + ch * 2) * BOX + RB * 128;
    u32x4 ha0 = *(LAS u32x4*)(lds + L_HS + (rb2 * 2 + (n >> 3)) * 192 + g * 16);
    u32x4 ha1 = *(LAS u32x4*)(lds + L_HS + (rb2 * 2 + (n >> 3)) * 192 + 64 + g * 16);
    asm volatile("" : "+v"(st_d), "+v"(st_o), "+v"(tr_d), "+v"(tr_o), "+v"(og_off), "+v"(wt_off), "+v"(sb_vo), "+v"(ha0), "+v"(ha1));
    asm volatile("" : "+s"(sb_so));
    SCHED();
    __builtin_amdgcn_s_setprio(0);
    lds_wait<C_OLD, 12, 2>(cbase);
    u32x4 wl[6];
#pragma unroll
    for (int i = 0; i < 2; ++i) wl[i] = ld128<NT>(wdr, d_vo, d_so + 8 * i * (D * 2));
#pragma unroll
    for (int i = 0; i < 4; ++i) wl[2 + i] = ld128<NT>(wor, o_vo + (i & 1) * 2 * (D * 2), o_so + (i & 2) * 2 * (D * 2));
    SCHED();
    __builtin_amdgcn_s_setprio(2);
    wg_barrier();
#pragma unroll
    for (int i = 0; i < 2; ++i) *(LAS u32x4*)(lds + st_d + i * BLK) = wl[i];
#pragma unroll
    for (int i = 0; i < 4; ++i) *(LAS u32x4*)(lds + st_o + i * BLK) = wl[2 + i];
    lds_signal<C_WO>(cbase);
    lds_wait<C_WO, 4, 0>(cbase);
    u32x4 bd[4], bw[8];
#pragma unroll
    for (int i = 0; i < 4; ++i) bd[i] = tr_frag(lds + tr_d + (i >> 1) * 4 * BLK + (i & 1) * 32);
#pragma unroll
    for (int i = 0; i < 8; ++i) bw[i] = tr_frag(lds + tr_o + (i >> 1) * 4 * BLK + (i & 1) * 32);
    SCHED();
    const f32x4 z4 = {0.f, 0.f, 0.f, 0.f};
    const f32x4 da0 = mfma16(ha1, bd[2], mfma16(ha0, bd[0], z4)), da1 = mfma16(ha1, bd[3], mfma16(ha0, bd[1], z4));
    const float dn = (g & 1) ? da1[0] : da0[0];
    float dn1 = dpp<XOR8>(dn);
    u32 w2 = rec_word(dn, dn1, 2), w2t = w2 + tag;
    asm volatile("" : "+v"(dn1), "+v"(w2), "+v"(w2t), "+v"(bw[0]), "+v"(bw[1]), "+v"(bw[2]), "+v"(bw[3]), "+v"(bw[4]), "+v"(bw[5]), "+v"(bw[6]), "+v"(bw[7]));
    wg_barrier();
    u32x4 oa[4];
#pragma unroll
    for (int ks = 0; ks < 4; ++ks) oa[ks] = *(LAS u32x4*)(lds + og_off + ks * 64);
    const f32x4 wt = *(LAS f32x4*)(lds + wt_off);
    SCHED();
    f32x4 a0 = z4, a1 = z4;
#pragma unroll
    for (int ks = 0; ks < 4; ++ks) {
      a0 = mfma16(oa[ks], bw[2 * ks], a0);
      a1 = mfma16(oa[ks], bw[2 * ks + 1], a1);
    }
    float s0 = a0[0] * wt[0];
    s0 = fmaf(a0[1], wt[1], s0); s0 = fmaf(a0[2], wt[2], s0); s0 = fmaf(a0[3], wt[3], s0);
    PIN(s0);
    float s1 = a1[0] * wt[0];
    s1 = fmaf(a1[1], wt[1], s1); s1 = fmaf(a1[2], wt[2], s1); s1 = fmaf(a1[3], wt[3], s1);
    const float sv = fold16(s0, s1);
    const u32 r0 = rec_word(sv, dn1, 0), r1 = rec_word(dpp<XOR8>(sv), dn1, 1);
    __builtin_amdgcn_raw_buffer_store_b128(u32x4{r0, r1, w2, r0 + r1 + w2t}, wsr, sb_vo, sb_so, SC1);
  }
}

extern "C" void kernel_launch(void* const* d_in, const int* in_sizes, int n_in, void* d_out, int out_size, void* d_ws, size_t ws_size,
                              hipStream_t stream) {
  Args a;
  a.kc = (const u16*)d_in[1];
  a.vc = (const u16*)d_in[2];
  a.x = (const u16*)d_in[0];
  a.wq = (const u16*)d_in[3];
  a.wgate = (const u16*)d_in[6];
  a.wup = (const u16*)d_in[7];
  a.wdown = (const u16*)d_in[8];
  a.wo = (const u16*)d_in[4];
  a.out = (u16*)d_out;
  a.ws = (char*)d_ws;
  fused_decode<<<256, 1024, 0, stream>>>(a);
}
```
